```python
import torch

def _softmax(x, axis=-1):
    return torch.softmax(x, dim=axis)
def _gelu(x):
    return torch.nn.functional.gelu(x, approximate="tanh")
def _silu(x):
    return torch.nn.functional.silu(x)
def _sigmoid(x):
    return torch.sigmoid(x)
def _layernorm(x, axis=-1, eps=1e-5):
    m = x.mean(dim=axis, keepdim=True)
    v = x.var(dim=axis, keepdim=True, unbiased=False)
    return (x - m) / torch.sqrt(v + eps)
def _scatter_add(tgt, idx, src, axis=0):
    return tgt.clone().index_add_(axis, idx, src)
def _segment_sum(src, seg, n):
    out = torch.zeros((n,) + src.shape[1:], dtype=src.dtype)
    return out.index_add_(0, seg, src)
def _pad(x, pw):
    flat = tuple(v for pair in reversed(pw) for v in pair)
    return torch.nn.functional.pad(x, flat)

kernel_name = 'standalone__paged_block_table_kv_radixattention__decode'

def setup_inputs():

    _w = (lambda a: (lambda t: t.to(torch.bfloat16) if t.is_floating_point() else t)(torch.as_tensor(a)))
    B, H, Hkv, Dh = 8, 32, 8, 128
    Lk = 8192
    bs = 16
    nb = max(2, Lk // bs)
    P = max(1, nb // 2)
    n_phys = P + B * (nb - P)
    shared = torch.arange(P, dtype=torch.int64)
    priv = P + torch.arange(B * (nb - P), dtype=torch.int64).reshape(B, nb - P)
    bt = torch.cat([torch.tile(shared[None, :], (B, 1)), priv], dim=1)
    ctx = nb * bs - (torch.arange(1, B + 1, dtype=torch.int64) * 2654435761) % max(1, (nb * bs - P * bs) // 2)
    return {
        "q": _w((lambda *s: torch.randn(*s, dtype=torch.bfloat16) * 0.8)(B, H, Dh)),
        "k_cache": _w((lambda *s: torch.randn(*s, dtype=torch.bfloat16) * 0.8)(n_phys, bs, Hkv, Dh)),
        "v_cache": _w((lambda *s: torch.randn(*s, dtype=torch.bfloat16) * 0.05)(n_phys, bs, Hkv, Dh)),
        "block_table": _w(bt.to(torch.int64)),
        "ctx_lens": _w(torch.as_tensor(ctx).to(torch.int64)),
    }

def reference(q, k_cache, v_cache, block_table, ctx_lens):
    B, H, Dh = q.shape
    n_phys, bs, Hkv, _ = k_cache.shape
    nb = block_table.shape[1]
    Lk = nb * bs
    rep = H // Hkv
    k = k_cache[block_table].reshape(B, Lk, Hkv, Dh)
    v = v_cache[block_table].reshape(B, Lk, Hkv, Dh)
    k = (lambda x, n, axis: torch.repeat_interleave(x, n, dim=axis))(k, rep, axis=2)
    v = (lambda x, n, axis: torch.repeat_interleave(x, n, dim=axis))(v, rep, axis=2)
    scores = torch.einsum("bhd,bkhd->bhk", q, k) * (Dh ** -0.5)
    pos = torch.arange(Lk)
    valid = pos[None, :] < ctx_lens[:, None]
    scores = torch.where(valid[:, None, :], scores, -1e9)
    p = _softmax(scores, axis=-1)
    return torch.einsum("bhk,bkhd->bhd", p, v)
```

```cpp
#include <hip/hip_runtime.h>
#include <hip/hip_bf16.h>
#include <stdint.h>

#define NW 8
#define DEPTH 1
#define KV_NT 1
#define HEAD_MAJOR 1

typedef __attribute__((ext_vector_type(8))) __bf16 bf16x8;
typedef __attribute__((ext_vector_type(4))) __bf16 bf16x4;
typedef __attribute__((ext_vector_type(2))) __bf16 bf16x2;
typedef __attribute__((ext_vector_type(2))) float f32x2;
typedef __attribute__((ext_vector_type(4))) float f32x4;
typedef __attribute__((ext_vector_type(16))) float f32x16;
typedef __attribute__((ext_vector_type(4))) unsigned u32x4;
typedef __attribute__((address_space(3))) bf16x4* lds_bf16x4_p;

constexpr int NBATCH = 8, NH = 32, NHKV = 8, DH = 128, PAGE = 16, NBLK = 512;
constexpr int NSLOT = 32;
constexpr int NTHR = NW * 64;
constexpr int CPT = NBLK / NTHR;
constexpr int MAXU = NBATCH * NBLK / NSLOT;
constexpr int PAGE_BYTES = PAGE * NHKV * DH * 2;
constexpr int TOK_BYTES = NHKV * DH * 2;
constexpr float SCALE_LOG2E = 0.08838834764831845f * 1.4426950408889634f;

constexpr int RING_BYTES = NW * DEPTH * 16384;
constexpr int OROW = 528;
constexpr int OBUF_BYTES = NW * 32 * OROW;
constexpr int BIG_BYTES = RING_BYTES > OBUF_BYTES ? RING_BYTES : OBUF_BYTES;
constexpr int L_CNT = BIG_BYTES;
constexpr int L_LIST = L_CNT + 64 * 4;
constexpr int L_M = L_LIST + MAXU * 8;
constexpr int L_L = L_M + NW * 32 * 4;
constexpr int LDS_BYTES = L_L + NW * 32 * 4;
static_assert(LDS_BYTES <= 163840, "lds");

constexpr size_t WS_ML = 0;
constexpr size_t WS_PO = (size_t)NHKV * 32 * NSLOT * 8;

__device__ __forceinline__ float swap32_max(float x) {
  auto r = __builtin_amdgcn_permlane32_swap(__float_as_uint(x), __float_as_uint(x), false, false);
  return fmaxf(__uint_as_float(r[0]), __uint_as_float(r[1]));
}
__device__ __forceinline__ float swap32_sum(float x) {
  auto r = __builtin_amdgcn_permlane32_swap(__float_as_uint(x), __float_as_uint(x), false, false);
  return __uint_as_float(r[0]) + __uint_as_float(r[1]);
}

#define NTS " nt"
__device__ __forceinline__ void dma_page(unsigned ldsaddr, u32x4 srd, unsigned soff, unsigned v0, unsigned v1, unsigned v2, unsigned v3) {
  asm volatile(
      "s_nop 4\n\t"
      "s_mov_b32 m0, %0\n\t"
      "s_nop 0\n\t"
      "buffer_load_dwordx4 %3, %1, %2 offen" NTS " lds\n\t"
      "buffer_load_dwordx4 %4, %1, %2 offen offset:1024" NTS " lds\n\t"
      "buffer_load_dwordx4 %5, %1, %2 offen offset:2048" NTS " lds\n\t"
      "buffer_load_dwordx4 %6, %1, %2 offen offset:3072" NTS " lds"
      :: "s"(ldsaddr), "s"(srd), "s"(soff), "v"(v0), "v"(v1), "v"(v2), "v"(v3) : "memory");
}
#define WAITVM(n) asm volatile("s_waitcnt vmcnt(" #n ")" ::: "memory")
__device__ __forceinline__ void wait_vm4(int n4) {
  if (DEPTH == 1) {
    if (n4 >= 2) WAITVM(8); else if (n4 == 1) WAITVM(4); else WAITVM(0);
  } else if (DEPTH == 2) {
    if (n4 >= 6) WAITVM(24); else if (n4 == 5) WAITVM(20); else if (n4 == 4) WAITVM(16); else if (n4 == 3) WAITVM(12);
    else if (n4 == 2) WAITVM(8); else if (n4 == 1) WAITVM(4); else WAITVM(0);
  } else {
    if (n4 >= 14) WAITVM(56); else if (n4 >= 12) WAITVM(48); else if (n4 >= 10) WAITVM(40); else if (n4 >= 8) WAITVM(32);
    else if (n4 >= 6) WAITVM(24); else if (n4 == 5) WAITVM(20); else if (n4 == 4) WAITVM(16); else if (n4 == 3) WAITVM(12);
    else if (n4 == 2) WAITVM(8); else if (n4 == 1) WAITVM(4); else WAITVM(0);
  }
}

__global__ void __launch_bounds__(NTHR, NW / 4)
attn_main(const __hip_bfloat16* __restrict__ q, const void* __restrict__ kc, const void* __restrict__ vc,
          const int* __restrict__ bt, const int* __restrict__ ctx, unsigned char* __restrict__ ws, unsigned kv_bytes) {
  __shared__ __attribute__((aligned(16))) char smem[LDS_BYTES];
  const int tid = threadIdx.x, lane = tid & 63;
  const int wave = __builtin_amdgcn_readfirstlane(tid >> 6);
  const int head = blockIdx.x & 7, slot = blockIdx.x >> 3;
  const int col = lane & 31, hh = lane >> 5;

  int id[CPT][NBATCH];
#pragma unroll
  for (int c = 0; c < CPT; ++c)
#pragma unroll
    for (int b = 0; b < NBATCH; ++b) id[c][b] = bt[b * NBLK + (wave * CPT + c) * 64 + lane];
  int cl[NBATCH];
#pragma unroll
  for (int b = 0; b < NBATCH; ++b) cl[b] = ctx[b];
  bf16x8 qf[8];
  {
    const __hip_bfloat16* qp = q + ((col >> 2) * NH + head * 4 + (col & 3)) * DH + 8 * hh;
#pragma unroll
    for (int s = 0; s < 8; ++s) qf[s] = *reinterpret_cast<const bf16x8*>(qp + 16 * s);
  }
  int myc = 0; bool myzero = false;
#pragma unroll
  for (int b = 0; b < NBATCH; ++b) {
    const bool z = cl[b] <= 0;
    cl[b] = z ? NBLK * PAGE : min(cl[b], NBLK * PAGE);
    if ((col >> 2) == b) { myc = cl[b]; myzero = z; }
  }
  unsigned msk[CPT][NBATCH];
  unsigned long long bal[CPT][NBATCH];
  int* cnt = reinterpret_cast<int*>(smem + L_CNT);
#pragma unroll
  for (int c = 0; c < CPT; ++c) {
    const int j = (wave * CPT + c) * 64 + lane;
#pragma unroll
    for (int b = 0; b < NBATCH; ++b) if (!(j * PAGE < cl[b])) id[c][b] = -1 - b;
#pragma unroll
    for (int b = 0; b < NBATCH; ++b) msk[c][b] = 1u << b;
#pragma unroll
    for (int b = 0; b < NBATCH; ++b)
#pragma unroll
      for (int e = b + 1; e < NBATCH; ++e) {
        const bool eq = id[c][b] == id[c][e];
        msk[c][b] |= eq ? (1u << e) : 0u;
        msk[c][e] |= eq ? (1u << b) : 0u;
      }
#pragma unroll
    for (int b = 0; b < NBATCH; ++b) {
      const bool lead = (j * PAGE < cl[b]) && (msk[c][b] & ((1u << b) - 1u)) == 0u;
      bal[c][b] = __ballot(lead);
      if (!lead) msk[c][b] = 0u;
    }
    int v = 0;
#pragma unroll
    for (int b = 0; b < NBATCH; ++b) if (lane == b) v = __popcll(bal[c][b]);
    if (lane < NBATCH) cnt[lane * 8 + wave * CPT + c] = v;
  }
  __syncthreads();
  int lo, n;
  {
    const int own = cnt[lane];
    int incl = own;
#pragma unroll
    for (int o = 1; o < 64; o <<= 1) { const int t = __shfl_up(incl, o); if (lane >= o) incl += t; }
    const int excl = incl - own;
    const int U = __builtin_amdgcn_readlane(incl, 63);
    lo = (slot * U) / NSLOT;
    n = ((slot + 1) * U) / NSLOT - lo;
    uint2* list = reinterpret_cast<uint2*>(smem + L_LIST);
#pragma unroll
    for (int c = 0; c < CPT; ++c) {
      const int j = (wave * CPT + c) * 64 + lane;
#pragma unroll
      for (int b = 0; b < NBATCH; ++b) {
        const int base = __builtin_amdgcn_readlane(excl, b * 8 + wave * CPT + c) - lo;
        const int u = base + (int)__builtin_amdgcn_mbcnt_hi((unsigned)(bal[c][b] >> 32), __builtin_amdgcn_mbcnt_lo((unsigned)bal[c][b], 0u));
        if (msk[c][b] != 0u && (unsigned)u < (unsigned)n) list[u] = make_uint2((unsigned)id[c][b], msk[c][b] | ((unsigned)j << 8));
      }
    }
  }
  __syncthreads();
  const int ntiles = (n + 1) >> 1;
  const int nt = (ntiles > wave) ? (ntiles - wave + NW - 1) / NW : 0;
  const bool lastodd = (n & 1) && (nt > 0) && (wave + NW * (nt - 1) == ntiles - 1);
  uint4 ent = make_uint4(0u, 0u, 0u, 0u);
  if (lane < nt) ent = *reinterpret_cast<const uint4*>(smem + L_LIST + 16 * (wave + NW * lane));
  if (lastodd && lane == nt - 1) { ent.z = 0u; ent.w = 0u; }
  if (myzero) {
#pragma unroll
    for (int s = 0; s < 8; ++s) qf[s] = bf16x8{};
  }
#pragma unroll
  for (int s = 0; s < 8; ++s) asm volatile("" : "+v"(qf[s]));

  const uint64_t kb = (uint64_t)kc, vb = (uint64_t)vc;
  const u32x4 srdK = {(unsigned)kb, (unsigned)(kb >> 32) & 0xffffu, kv_bytes, 0x00020000u};
  const u32x4 srdV = {(unsigned)vb, (unsigned)(vb >> 32) & 0xffffu, kv_bytes, 0x00020000u};
  const unsigned g4 = lane >> 4, p16 = lane & 15;
  unsigned vk[4], vv[4];
#pragma unroll
  for (int i = 0; i < 4; ++i) {
    vk[i] = (4 * i + g4) * TOK_BYTES + 16 * (p16 ^ (4 * i + g4)) - 1024 * i;
    vv[i] = (4 * i + g4) * TOK_BYTES + 16 * (p16 ^ (g4 << 2)) - 1024 * i;
  }
  const unsigned sbase = (unsigned)(size_t)smem;
  const unsigned ring = sbase + wave * (DEPTH * 16384);
  const unsigned hoff = head * (DH * 2);

  auto issueK = [&](int i, int slotd) {
    const unsigned pa = __builtin_amdgcn_readlane(ent.x, i) * PAGE_BYTES + hoff;
    dma_page(ring + slotd * 16384, srdK, pa, vk[0], vk[1], vk[2], vk[3]);
    if (!(lastodd && i == nt - 1)) {
      const unsigned pb = __builtin_amdgcn_readlane(ent.z, i) * PAGE_BYTES + hoff;
      dma_page(ring + slotd * 16384 + 4096, srdK, pb, vk[0], vk[1], vk[2], vk[3]);
    }
  };
  auto issueV = [&](int i, int slotd) {
    const unsigned pa = __builtin_amdgcn_readlane(ent.x, i) * PAGE_BYTES + hoff;
    dma_page(ring + slotd * 16384 + 8192, srdV, pa, vv[0], vv[1], vv[2], vv[3]);
    if (!(lastodd && i == nt - 1)) {
      const unsigned pb = __builtin_amdgcn_readlane(ent.z, i) * PAGE_BYTES + hoff;
      dma_page(ring + slotd * 16384 + 12288, srdV, pb, vv[0], vv[1], vv[2], vv[3]);
    }
  };
#pragma unroll
  for (int d = 0; d < DEPTH; ++d) if (d < nt) { issueK(d, d); issueV(d, d); }

  const unsigned krow = col * 256;
  const unsigned tq = (lane & 15) >> 2, tp = lane & 3, blk = (lane >> 4) & 1;
  const unsigned vrow = 8192 + (4 * hh + tq) * 256 + 8 * (tp & 1);

  f32x16 acc[4];
#pragma unroll
  for (int c = 0; c < 4; ++c) acc[c] = f32x16{};
  float m = -INFINITY, lsum = 0.f;
  const int tot4 = 2 * nt - (lastodd ? 1 : 0);

  int sd = 0;
  for (int i = 0; i < nt; ++i) {
    char* rb = smem + wave * (DEPTH * 16384) + sd * 16384;
    const int ahead = min(tot4, 2 * (i + DEPTH)) - min(tot4, 2 * (i + 1));
    const int npi = min(tot4 - 2 * i, 2);
    const int npn = max(0, min(tot4 - 2 * (i + DEPTH), 2));
    wait_vm4(npi + 2 * ahead);
    bf16x8 kf[8];
#pragma unroll
    for (int s = 0; s < 8; ++s) kf[s] = *reinterpret_cast<const bf16x8*>(rb + krow + 16 * ((2 * s + hh) ^ (col & 15)));
    asm volatile("s_waitcnt lgkmcnt(0)" ::: "memory");
    if (i + DEPTH < nt) issueK(i + DEPTH, sd);
    f32x16 sc = f32x16{};
#pragma unroll
    for (int s = 0; s < 8; ++s) sc = __builtin_amdgcn_mfma_f32_32x32x16_bf16(kf[s], qf[s], sc, 0, 0, 0);
    const unsigned ea = __builtin_amdgcn_readlane(ent.y, i), eb = __builtin_amdgcn_readlane(ent.w, i);
    const int nva = (((ea >> (col >> 2)) & 1u) ? myc - (int)(ea >> 8) * PAGE : 0) - 4 * hh;
    const int nvb = (((eb >> (col >> 2)) & 1u) ? myc - (int)(eb >> 8) * PAGE : 0) - 4 * hh;
    float mx = -INFINITY;
#pragma unroll
    for (int r = 0; r < 16; ++r) {
      const int t = (r & 3) + 8 * ((r >> 2) & 1);
      sc[r] = (t < (r < 8 ? nva : nvb)) ? sc[r] : -INFINITY;
      mx = fmaxf(mx, sc[r]);
    }
    mx = swap32_max(mx);
    const float mn = fmaxf(m, mx);
    const float msub = (mn == -INFINITY) ? 0.f : mn * SCALE_LOG2E;
    const float alpha = __builtin_amdgcn_exp2f(m * SCALE_LOG2E - msub);
    m = mn;
    float ps = 0.f;
#pragma unroll
    for (int r = 0; r < 16; ++r) { sc[r] = __builtin_amdgcn_exp2f(fmaf(sc[r], SCALE_LOG2E, -msub)); ps += sc[r]; }
    lsum = fmaf(lsum, alpha, ps);
    bf16x8 pb[2];
#pragma unroll
    for (int s = 0; s < 2; ++s)
#pragma unroll
      for (int k = 0; k < 4; ++k) {
        const bf16x2 t = __builtin_convertvector((f32x2{sc[8 * s + 2 * k], sc[8 * s + 2 * k + 1]}), bf16x2);
        pb[s][2 * k] = t[0]; pb[s][2 * k + 1] = t[1];
      }
#pragma unroll
    for (int c = 0; c < 4; ++c) acc[c] *= alpha;
    wait_vm4(2 * ahead + npn);
    bf16x8 vf[2][4];
#pragma unroll
    for (int s = 0; s < 2; ++s)
#pragma unroll
      for (int c = 0; c < 4; ++c) {
        char* a = rb + vrow + 16 * ((4 * c + 2 * blk + (tp >> 1)) ^ (tq << 2)) + s * 4096;
        const bf16x4 x = __builtin_amdgcn_ds_read_tr16_b64_v4bf16((lds_bf16x4_p)(a));
        const bf16x4 y = __builtin_amdgcn_ds_read_tr16_b64_v4bf16((lds_bf16x4_p)(a + 2048));
        vf[s][c] = __builtin_shufflevector(x, y, 0, 1, 2, 3, 4, 5, 6, 7);
      }
    asm volatile("s_waitcnt lgkmcnt(0)" ::: "memory");
    if (i + DEPTH < nt) issueV(i + DEPTH, sd);
#pragma unroll
    for (int c = 0; c < 4; ++c) acc[c] = __builtin_amdgcn_mfma_f32_32x32x16_bf16(vf[0][c], pb[0], acc[c], 0, 0, 0);
    if (npi == 2) {
#pragma unroll
      for (int c = 0; c < 4; ++c) acc[c] = __builtin_amdgcn_mfma_f32_32x32x16_bf16(vf[1][c], pb[1], acc[c], 0, 0, 0);
    }
    sd = (sd + 1 == DEPTH) ? 0 : sd + 1;
  }

  float* sm = reinterpret_cast<float*>(smem + L_M);
  float* sl = reinterpret_cast<float*>(smem + L_L);
  const float ltot = swap32_sum(lsum);
  const float ms = m * SCALE_LOG2E;
  if (lane < 32) { sm[wave * 32 + lane] = ms; sl[wave * 32 + lane] = ltot; }
  __syncthreads();
  {
    float M = -INFINITY;
#pragma unroll
    for (int w = 0; w < NW; ++w) M = fmaxf(M, sm[w * 32 + col]);
    if (M != -INFINITY) {
      const float f = __builtin_amdgcn_exp2f(ms - M);
      char* ob = smem + (wave * 32 + col) * OROW + 16 * hh;
#pragma unroll
      for (int c = 0; c < 4; ++c)
#pragma unroll
        for (int g = 0; g < 4; ++g)
          *reinterpret_cast<f32x4*>(ob + 128 * c + 32 * g) = f32x4{acc[c][4 * g] * f, acc[c][4 * g + 1] * f, acc[c][4 * g + 2] * f, acc[c][4 * g + 3] * f};
    }
    if (tid < 32) {
      float L = 0.f;
      const float Ms = (M == -INFINITY) ? 0.f : M;
#pragma unroll
      for (int w = 0; w < NW; ++w) L = fmaf(sl[w * 32 + col], __builtin_amdgcn_exp2f(sm[w * 32 + col] - Ms), L);
      reinterpret_cast<float2*>(ws + WS_ML)[(head * 32 + col) * NSLOT + slot] = make_float2(M, L);
    }
  }
  __syncthreads();
#pragma unroll
  for (int it = 0; it < 1024 / NTHR; ++it) {
    const int cc = (tid >> 5) + it * (NTHR / 32), d4 = tid & 31;
    float M = -INFINITY;
#pragma unroll
    for (int w = 0; w < NW; ++w) M = fmaxf(M, sm[w * 32 + cc]);
    if (M != -INFINITY) {
      f32x4 o = f32x4{};
#pragma unroll
      for (int w = 0; w < NW; ++w) o += *reinterpret_cast<const f32x4*>(smem + (w * 32 + cc) * OROW + 16 * d4);
      *reinterpret_cast<f32x4*>(ws + WS_PO + ((size_t)((head * 32 + cc) * NSLOT + slot) * DH + 4 * d4) * 4) = o;
    }
  }
}

__global__ void __launch_bounds__(256) attn_combine(const unsigned char* __restrict__ ws, __hip_bfloat16* __restrict__ out) {
  __shared__ __attribute__((aligned(16))) float red[8][DH];
  const int head = blockIdx.x & 7, col = blockIdx.x >> 3;
  const int tid = threadIdx.x, s = tid & 31, grp = tid >> 5;
  const int row = head * 32 + col;
  const float2 ml = reinterpret_cast<const float2*>(ws + WS_ML)[row * NSLOT + s];
  float M = ml.x;
#pragma unroll
  for (int o = 16; o >= 1; o >>= 1) M = fmaxf(M, __shfl_xor(M, o));
  const float w = (ml.x == -INFINITY) ? 0.f : __builtin_amdgcn_exp2f(ml.x - M);
  float L = w * ml.y;
#pragma unroll
  for (int o = 16; o >= 1; o >>= 1) L += __shfl_xor(L, o);
  f32x4 part[4]; float wk[4];
#pragma unroll
  for (int k = 0; k < 4; ++k) {
    const int sl = grp + 8 * k;
    const float mk = __shfl(ml.x, (tid & 32) + sl);
    wk[k] = __shfl(w, (tid & 32) + sl);
    part[k] = f32x4{};
    if (mk != -INFINITY) part[k] = *reinterpret_cast<const f32x4*>(ws + WS_PO + ((size_t)(row * NSLOT + sl) * DH + 4 * s) * 4);
  }
  f32x4 o = f32x4{};
#pragma unroll
  for (int k = 0; k < 4; ++k) o += part[k] * wk[k];
  *reinterpret_cast<f32x4*>(&red[grp][4 * s]) = o;
  __syncthreads();
  if (tid < 64) {
    float a = 0.f, b = 0.f;
#pragma unroll
    for (int g = 0; g < 8; ++g) { a += red[g][2 * tid]; b += red[g][2 * tid + 1]; }
    const float inv = 1.f / L;
    const bf16x2 r = __builtin_convertvector((f32x2{a * inv, b * inv}), bf16x2);
    *reinterpret_cast<bf16x2*>(out + ((col >> 2) * NH + head * 4 + (col & 3)) * DH + 2 * tid) = r;
  }
}

extern "C" void kernel_launch(void* const* d_in, const int* in_sizes, int n_in, void* d_out, int out_size, void* d_ws, size_t ws_size,
                              hipStream_t stream) {
  const __hip_bfloat16* q = (const __hip_bfloat16*)d_in[0];
  const unsigned kv_bytes = (unsigned)in_sizes[1] * 2u;
  attn_main<<<NHKV * NSLOT, NTHR, 0, stream>>>(q, d_in[1], d_in[2], (const int*)d_in[3], (const int*)d_in[4], (unsigned char*)d_ws, kv_bytes);
  attn_combine<<<NHKV * 32, 256, 0, stream>>>((const unsigned char*)d_ws, (__hip_bfloat16*)d_out);
}
```

```cpp
#include <hip/hip_runtime.h>
#include <hip/hip_bf16.h>
#include <stdint.h>

#define NW 8
#define DEPTH 1
#define HEAD_MAJOR 1

typedef __attribute__((ext_vector_type(8))) __bf16 bf16x8;
typedef __attribute__((ext_vector_type(4))) __bf16 bf16x4;
typedef __attribute__((ext_vector_type(2))) __bf16 bf16x2;
typedef __attribute__((ext_vector_type(2))) float f32x2;
typedef __attribute__((ext_vector_type(4))) float f32x4;
typedef __attribute__((ext_vector_type(16))) float f32x16;
typedef __attribute__((ext_vector_type(4))) unsigned u32x4;
typedef __attribute__((address_space(3))) bf16x4* lds_bf16x4_p;

constexpr int NBATCH = 8, NH = 32, NHKV = 8, DH = 128, PAGE = 16, NBLK = 512;
constexpr int NSLOT = 32;
constexpr int NTHR = NW * 64;
constexpr int CPT = NBLK / NTHR;
constexpr int MAXU = NBATCH * NBLK / NSLOT;
constexpr int PAGE_BYTES = PAGE * NHKV * DH * 2;
constexpr int TOK_BYTES = NHKV * DH * 2;
constexpr float SCALE_LOG2E = 0.08838834764831845f * 1.4426950408889634f;

constexpr int RING_BYTES = NW * DEPTH * 16384;
constexpr int OROW = 528;
constexpr int OBUF_BYTES = NW * 32 * OROW;
constexpr int BIG_BYTES = RING_BYTES > OBUF_BYTES ? RING_BYTES : OBUF_BYTES;
constexpr int L_CNT = BIG_BYTES;
constexpr int L_LIST = L_CNT + 64 * 4;
constexpr int L_M = L_LIST + MAXU * 8;
constexpr int L_L = L_M + NW * 32 * 4;
constexpr int LDS_BYTES = L_L + NW * 32 * 4;
static_assert(LDS_BYTES <= 163840, "lds");

constexpr size_t WS_ML = 0;
constexpr size_t WS_PO = (size_t)NHKV * 32 * NSLOT * 8;

__device__ __forceinline__ float swap32_max(float x) {
  auto r = __builtin_amdgcn_permlane32_swap(__float_as_uint(x), __float_as_uint(x), false, false);
  return fmaxf(__uint_as_float(r[0]), __uint_as_float(r[1]));
}
__device__ __forceinline__ float swap32_sum(float x) {
  auto r = __builtin_amdgcn_permlane32_swap(__float_as_uint(x), __float_as_uint(x), false, false);
  return __uint_as_float(r[0]) + __uint_as_float(r[1]);
}

#define K_POL ""
#define V_POL " nt"
#define DMA_PAGE_FN(NAME, POL) \
__device__ __forceinline__ void NAME(unsigned ldsaddr, u32x4 srd, unsigned soff, unsigned v0, unsigned v1, unsigned v2, unsigned v3) { \
  asm volatile( \
      "s_nop 4\n\t" \
      "s_mov_b32 m0, %0\n\t" \
      "s_nop 0\n\t" \
      "buffer_load_dwordx4 %3, %1, %2 offen" POL " lds\n\t" \
      "buffer_load_dwordx4 %4, %1, %2 offen offset:1024" POL " lds\n\t" \
      "buffer_load_dwordx4 %5, %1, %2 offen offset:2048" POL " lds\n\t" \
      "buffer_load_dwordx4 %6, %1, %2 offen offset:3072" POL " lds" \
      :: "s"(ldsaddr), "s"(srd), "s"(soff), "v"(v0), "v"(v1), "v"(v2), "v"(v3) : "memory"); \
}
DMA_PAGE_FN(dma_pageK, K_POL)
DMA_PAGE_FN(dma_pageV, V_POL)
#define WAITVM(n) asm volatile("s_waitcnt vmcnt(" #n ")" ::: "memory")
__device__ __forceinline__ void wait_vm4(int n4) {
  if (DEPTH == 1) {
    if (n4 >= 2) WAITVM(8); else if (n4 == 1) WAITVM(4); else WAITVM(0);
  } else if (DEPTH == 2) {
    if (n4 >= 6) WAITVM(24); else if (n4 == 5) WAITVM(20); else if (n4 == 4) WAITVM(16); else if (n4 == 3) WAITVM(12);
    else if (n4 == 2) WAITVM(8); else if (n4 == 1) WAITVM(4); else WAITVM(0);
  } else {
    if (n4 >= 14) WAITVM(56); else if (n4 >= 12) WAITVM(48); else if (n4 >= 10) WAITVM(40); else if (n4 >= 8) WAITVM(32);
    else if (n4 >= 6) WAITVM(24); else if (n4 == 5) WAITVM(20); else if (n4 == 4) WAITVM(16); else if (n4 == 3) WAITVM(12);
    else if (n4 == 2) WAITVM(8); else if (n4 == 1) WAITVM(4); else WAITVM(0);
  }
}

__global__ void __launch_bounds__(NTHR, NW / 4)
attn_main(const __hip_bfloat16* __restrict__ q, const void* __restrict__ kc, const void* __restrict__ vc,
          const int* __restrict__ bt, const int* __restrict__ ctx, unsigned char* __restrict__ ws, unsigned kv_bytes) {
  __shared__ __attribute__((aligned(16))) char smem[LDS_BYTES];
  const int tid = threadIdx.x, lane = tid & 63;
  const int wave = __builtin_amdgcn_readfirstlane(tid >> 6);
  const int head = blockIdx.x & 7, slot = blockIdx.x >> 3;
  const int col = lane & 31, hh = lane >> 5;

  int id[CPT][NBATCH];
#pragma unroll
  for (int c = 0; c < CPT; ++c)
#pragma unroll
    for (int b = 0; b < NBATCH; ++b) id[c][b] = bt[b * NBLK + (wave * CPT + c) * 64 + lane];
  int cl[NBATCH];
#pragma unroll
  for (int b = 0; b < NBATCH; ++b) cl[b] = ctx[b];
  bf16x8 qf[8];
  {
    const __hip_bfloat16* qp = q + ((col >> 2) * NH + head * 4 + (col & 3)) * DH + 8 * hh;
#pragma unroll
    for (int s = 0; s < 8; ++s) qf[s] = *reinterpret_cast<const bf16x8*>(qp + 16 * s);
  }
  int myc = 0; bool myzero = false;
#pragma unroll
  for (int b = 0; b < NBATCH; ++b) {
    const bool z = cl[b] <= 0;
    cl[b] = z ? NBLK * PAGE : min(cl[b], NBLK * PAGE);
    if ((col >> 2) == b) { myc = cl[b]; myzero = z; }
  }
  unsigned msk[CPT][NBATCH];
  unsigned long long bal[CPT][NBATCH];
  int* cnt = reinterpret_cast<int*>(smem + L_CNT);
#pragma unroll
  for (int c = 0; c < CPT; ++c) {
    const int j = (wave * CPT + c) * 64 + lane;
#pragma unroll
    for (int b = 0; b < NBATCH; ++b) if (!(j * PAGE < cl[b])) id[c][b] = -1 - b;
#pragma unroll
    for (int b = 0; b < NBATCH; ++b) msk[c][b] = 1u << b;
#pragma unroll
    for (int b = 0; b < NBATCH; ++b)
#pragma unroll
      for (int e = b + 1; e < NBATCH; ++e) {
        const bool eq = id[c][b] == id[c][e];
        msk[c][b] |= eq ? (1u << e) : 0u;
        msk[c][e] |= eq ? (1u << b) : 0u;
      }
#pragma unroll
    for (int b = 0; b < NBATCH; ++b) {
      const bool lead = (j * PAGE < cl[b]) && (msk[c][b] & ((1u << b) - 1u)) == 0u;
      bal[c][b] = __ballot(lead);
      if (!lead) msk[c][b] = 0u;
    }
    int v = 0;
#pragma unroll
    for (int b = 0; b < NBATCH; ++b) if (lane == b) v = __popcll(bal[c][b]);
    if (lane < NBATCH) cnt[lane * 8 + wave * CPT + c] = v;
  }
  __syncthreads();
  int lo, n;
  {
    const int own = cnt[lane];
    int incl = own;
#pragma unroll
    for (int o = 1; o < 64; o <<= 1) { const int t = __shfl_up(incl, o); if (lane >= o) incl += t; }
    const int excl = incl - own;
    const int U = __builtin_amdgcn_readlane(incl, 63);
    lo = (slot * U) / NSLOT;
    n = ((slot + 1) * U) / NSLOT - lo;
    uint2* list = reinterpret_cast<uint2*>(smem + L_LIST);
#pragma unroll
    for (int c = 0; c < CPT; ++c) {
      const int j = (wave * CPT + c) * 64 + lane;
#pragma unroll
      for (int b = 0; b < NBATCH; ++b) {
        const int base = __builtin_amdgcn_readlane(excl, b * 8 + wave * CPT + c) - lo;
        const int u = base + (int)__builtin_amdgcn_mbcnt_hi((unsigned)(bal[c][b] >> 32), __builtin_amdgcn_mbcnt_lo((unsigned)bal[c][b], 0u));
        if (msk[c][b] != 0u && (unsigned)u < (unsigned)n) list[u] = make_uint2((unsigned)id[c][b], msk[c][b] | ((unsigned)j << 8));
      }
    }
  }
  __syncthreads();
  const int ntiles = (n + 1) >> 1;
  const int nt = (ntiles > wave) ? (ntiles - wave + NW - 1) / NW : 0;
  const bool lastodd = (n & 1) && (nt > 0) && (wave + NW * (nt - 1) == ntiles - 1);
  uint4 ent = make_uint4(0u, 0u, 0u, 0u);
  if (lane < nt) ent = *reinterpret_cast<const uint4*>(smem + L_LIST + 16 * (wave + NW * lane));
  if (lastodd && lane == nt - 1) { ent.z = 0u; ent.w = 0u; }
  if (myzero) {
#pragma unroll
    for (int s = 0; s < 8; ++s) qf[s] = bf16x8{};
  }
#pragma unroll
  for (int s = 0; s < 8; ++s) asm volatile("" : "+v"(qf[s]));

  const uint64_t kb = (uint64_t)kc, vb = (uint64_t)vc;
  const u32x4 srdK = {(unsigned)kb, (unsigned)(kb >> 32) & 0xffffu, kv_bytes, 0x00020000u};
  const u32x4 srdV = {(unsigned)vb, (unsigned)(vb >> 32) & 0xffffu, kv_bytes, 0x00020000u};
  const unsigned g4 = lane >> 4, p16 = lane & 15;
  unsigned vk[4], vv[4];
#pragma unroll
  for (int i = 0; i < 4; ++i) {
    vk[i] = (4 * i + g4) * TOK_BYTES + 16 * (p16 ^ (4 * i + g4)) - 1024 * i;
    vv[i] = (4 * i + g4) * TOK_BYTES + 16 * (p16 ^ (g4 << 2)) - 1024 * i;
  }
  const unsigned sbase = (unsigned)(size_t)smem;
  const unsigned ring = sbase + wave * (DEPTH * 16384);
  const unsigned hoff = head * (DH * 2);

  auto issueK = [&](int i, int slotd) {
    const unsigned pa = __builtin_amdgcn_readlane(ent.x, i) * PAGE_BYTES + hoff;
    dma_pageK(ring + slotd * 16384, srdK, pa, vk[0], vk[1], vk[2], vk[3]);
    if (!(lastodd && i == nt - 1)) {
      const unsigned pb = __builtin_amdgcn_readlane(ent.z, i) * PAGE_BYTES + hoff;
      dma_pageK(ring + slotd * 16384 + 4096, srdK, pb, vk[0], vk[1], vk[2], vk[3]);
    }
  };
  auto issueV = [&](int i, int slotd) {
    const unsigned pa = __builtin_amdgcn_readlane(ent.x, i) * PAGE_BYTES + hoff;
    dma_pageV(ring + slotd * 16384 + 8192, srdV, pa, vv[0], vv[1], vv[2], vv[3]);
    if (!(lastodd && i == nt - 1)) {
      const unsigned pb = __builtin_amdgcn_readlane(ent.z, i) * PAGE_BYTES + hoff;
      dma_pageV(ring + slotd * 16384 + 12288, srdV, pb, vv[0], vv[1], vv[2], vv[3]);
    }
  };
#pragma unroll
  for (int d = 0; d < DEPTH; ++d) if (d < nt) { issueK(d, d); issueV(d, d); }

  const unsigned krow = col * 256;
  const unsigned tq = (lane & 15) >> 2, tp = lane & 3, blk = (lane >> 4) & 1;
  const unsigned vrow = 8192 + (4 * hh + tq) * 256 + 8 * (tp & 1);

  f32x16 acc[4];
#pragma unroll
  for (int c = 0; c < 4; ++c) acc[c] = f32x16{};
  float m = -INFINITY, lsum = 0.f;
  const int tot4 = 2 * nt - (lastodd ? 1 : 0);

  int sd = 0;
  for (int i = 0; i < nt; ++i) {
    char* rb = smem + wave * (DEPTH * 16384) + sd * 16384;
    const int ahead = min(tot4, 2 * (i + DEPTH)) - min(tot4, 2 * (i + 1));
    const int npi = min(tot4 - 2 * i, 2);
    const int npn = max(0, min(tot4 - 2 * (i + DEPTH), 2));
    wait_vm4(npi + 2 * ahead);
    bf16x8 kf[8];
#pragma unroll
    for (int s = 0; s < 8; ++s) kf[s] = *reinterpret_cast<const bf16x8*>(rb + krow + 16 * ((2 * s + hh) ^ (col & 15)));
    asm volatile("s_waitcnt lgkmcnt(0)" ::: "memory");
    if (i + DEPTH < nt) issueK(i + DEPTH, sd);
    f32x16 sc = f32x16{};
#pragma unroll
    for (int s = 0; s < 8; ++s) sc = __builtin_amdgcn_mfma_f32_32x32x16_bf16(kf[s], qf[s], sc, 0, 0, 0);
    const unsigned ea = __builtin_amdgcn_readlane(ent.y, i), eb = __builtin_amdgcn_readlane(ent.w, i);
    const int nva = (((ea >> (col >> 2)) & 1u) ? myc - (int)(ea >> 8) * PAGE : 0) - 4 * hh;
    const int nvb = (((eb >> (col >> 2)) & 1u) ? myc - (int)(eb >> 8) * PAGE : 0) - 4 * hh;
    float mx = -INFINITY;
#pragma unroll
    for (int r = 0; r < 16; ++r) {
      const int t = (r & 3) + 8 * ((r >> 2) & 1);
      sc[r] = (t < (r < 8 ? nva : nvb)) ? sc[r] : -INFINITY;
      mx = fmaxf(mx, sc[r]);
    }
    mx = swap32_max(mx);
    const float mn = fmaxf(m, mx);
    const float msub = (mn == -INFINITY) ? 0.f : mn * SCALE_LOG2E;
    const float alpha = __builtin_amdgcn_exp2f(m * SCALE_LOG2E - msub);
    m = mn;
    float ps = 0.f;
#pragma unroll
    for (int r = 0; r < 16; ++r) { sc[r] = __builtin_amdgcn_exp2f(fmaf(sc[r], SCALE_LOG2E, -msub)); ps += sc[r]; }
    lsum = fmaf(lsum, alpha, ps);
    bf16x8 pb[2];
#pragma unroll
    for (int s = 0; s < 2; ++s)
#pragma unroll
      for (int k = 0; k < 4; ++k) {
        const bf16x2 t = __builtin_convertvector((f32x2{sc[8 * s + 2 * k], sc[8 * s + 2 * k + 1]}), bf16x2);
        pb[s][2 * k] = t[0]; pb[s][2 * k + 1] = t[1];
      }
#pragma unroll
    for (int c = 0; c < 4; ++c) acc[c] *= alpha;
    wait_vm4(2 * ahead + npn);
    bf16x8 vf[2][4];
#pragma unroll
    for (int s = 0; s < 2; ++s)
#pragma unroll
      for (int c = 0; c < 4; ++c) {
        char* a = rb + vrow + 16 * ((4 * c + 2 * blk + (tp >> 1)) ^ (tq << 2)) + s * 4096;
        const bf16x4 x = __builtin_amdgcn_ds_read_tr16_b64_v4bf16((lds_bf16x4_p)(a));
        const bf16x4 y = __builtin_amdgcn_ds_read_tr16_b64_v4bf16((lds_bf16x4_p)(a + 2048));
        vf[s][c] = __builtin_shufflevector(x, y, 0, 1, 2, 3, 4, 5, 6, 7);
      }
    asm volatile("s_waitcnt lgkmcnt(0)" ::: "memory");
    if (i + DEPTH < nt) issueV(i + DEPTH, sd);
#pragma unroll
    for (int c = 0; c < 4; ++c) acc[c] = __builtin_amdgcn_mfma_f32_32x32x16_bf16(vf[0][c], pb[0], acc[c], 0, 0, 0);
    if (npi == 2) {
#pragma unroll
      for (int c = 0; c < 4; ++c) acc[c] = __builtin_amdgcn_mfma_f32_32x32x16_bf16(vf[1][c], pb[1], acc[c], 0, 0, 0);
    }
    sd = (sd + 1 == DEPTH) ? 0 : sd + 1;
  }

  float* sm = reinterpret_cast<float*>(smem + L_M);
  float* sl = reinterpret_cast<float*>(smem + L_L);
  const float ltot = swap32_sum(lsum);
  const float ms = m * SCALE_LOG2E;
  if (lane < 32) { sm[wave * 32 + lane] = ms; sl[wave * 32 + lane] = ltot; }
  __syncthreads();
  {
    float M = -INFINITY;
#pragma unroll
    for (int w = 0; w < NW; ++w) M = fmaxf(M, sm[w * 32 + col]);
    if (M != -INFINITY) {
      const float f = __builtin_amdgcn_exp2f(ms - M);
      char* ob = smem + (wave * 32 + col) * OROW + 16 * hh;
#pragma unroll
      for (int c = 0; c < 4; ++c)
#pragma unroll
        for (int g = 0; g < 4; ++g)
          *reinterpret_cast<f32x4*>(ob + 128 * c + 32 * g) = f32x4{acc[c][4 * g] * f, acc[c][4 * g + 1] * f, acc[c][4 * g + 2] * f, acc[c][4 * g + 3] * f};
    }
    if (tid < 32) {
      float L = 0.f;
      const float Ms = (M == -INFINITY) ? 0.f : M;
#pragma unroll
      for (int w = 0; w < NW; ++w) L = fmaf(sl[w * 32 + col], __builtin_amdgcn_exp2f(sm[w * 32 + col] - Ms), L);
      reinterpret_cast<float2*>(ws + WS_ML)[(head * 32 + col) * NSLOT + slot] = make_float2(M, L);
    }
  }
  __syncthreads();
#pragma unroll
  for (int it = 0; it < 1024 / NTHR; ++it) {
    const int cc = (tid >> 5) + it * (NTHR / 32), d4 = tid & 31;
    float M = -INFINITY;
#pragma unroll
    for (int w = 0; w < NW; ++w) M = fmaxf(M, sm[w * 32 + cc]);
    if (M != -INFINITY) {
      f32x4 o = f32x4{};
#pragma unroll
      for (int w = 0; w < NW; ++w) o += *reinterpret_cast<const f32x4*>(smem + (w * 32 + cc) * OROW + 16 * d4);
      *reinterpret_cast<f32x4*>(ws + WS_PO + ((size_t)((head * 32 + cc) * NSLOT + slot) * DH + 4 * d4) * 4) = o;
    }
  }
}

__global__ void __launch_bounds__(256) attn_combine(const unsigned char* __restrict__ ws, __hip_bfloat16* __restrict__ out) {
  __shared__ __attribute__((aligned(16))) float red[8][DH];
  const int head = blockIdx.x & 7, col = blockIdx.x >> 3;
  const int tid = threadIdx.x, s = tid & 31, grp = tid >> 5;
  const int row = head * 32 + col;
  const float2 ml = reinterpret_cast<const float2*>(ws + WS_ML)[row * NSLOT + s];
  float M = ml.x;
#pragma unroll
  for (int o = 16; o >= 1; o >>= 1) M = fmaxf(M, __shfl_xor(M, o));
  const float w = (ml.x == -INFINITY) ? 0.f : __builtin_amdgcn_exp2f(ml.x - M);
  float L = w * ml.y;
#pragma unroll
  for (int o = 16; o >= 1; o >>= 1) L += __shfl_xor(L, o);
  f32x4 part[4]; float wk[4];
#pragma unroll
  for (int k = 0; k < 4; ++k) {
    const int sl = grp + 8 * k;
    const float mk = __shfl(ml.x, (tid & 32) + sl);
    wk[k] = __shfl(w, (tid & 32) + sl);
    part[k] = f32x4{};
    if (mk != -INFINITY) part[k] = *reinterpret_cast<const f32x4*>(ws + WS_PO + ((size_t)(row * NSLOT + sl) * DH + 4 * s) * 4);
  }
  f32x4 o = f32x4{};
#pragma unroll
  for (int k = 0; k < 4; ++k) o += part[k] * wk[k];
  *reinterpret_cast<f32x4*>(&red[grp][4 * s]) = o;
  __syncthreads();
  if (tid < 64) {
    float a = 0.f, b = 0.f;
#pragma unroll
    for (int g = 0; g < 8; ++g) { a += red[g][2 * tid]; b += red[g][2 * tid + 1]; }
    const float inv = 1.f / L;
    const bf16x2 r = __builtin_convertvector((f32x2{a * inv, b * inv}), bf16x2);
    *reinterpret_cast<bf16x2*>(out + ((col >> 2) * NH + head * 4 + (col & 3)) * DH + 2 * tid) = r;
  }
}

extern "C" void kernel_launch(void* const* d_in, const int* in_sizes, int n_in, void* d_out, int out_size, void* d_ws, size_t ws_size,
                              hipStream_t stream) {
  const __hip_bfloat16* q = (const __hip_bfloat16*)d_in[0];
  const unsigned kv_bytes = (unsigned)in_sizes[1] * 2u;
  attn_main<<<NHKV * NSLOT, NTHR, 0, stream>>>(q, d_in[1], d_in[2], (const int*)d_in[3], (const int*)d_in[4], (unsigned char*)d_ws, kv_bytes);
  attn_combine<<<NHKV * 32, 256, 0, stream>>>((const unsigned char*)d_ws, (__hip_bfloat16*)d_out);
}
```

```cpp
#include <hip/hip_runtime.h>
#include <hip/hip_bf16.h>
#include <stdint.h>

#define NW 8
#define DEPTH 1
#define HEAD_ROT 0
#define HEAD_MAJOR 1

typedef __attribute__((ext_vector_type(8))) __bf16 bf16x8;
typedef __attribute__((ext_vector_type(4))) __bf16 bf16x4;
typedef __attribute__((ext_vector_type(2))) __bf16 bf16x2;
typedef __attribute__((ext_vector_type(2))) float f32x2;
typedef __attribute__((ext_vector_type(4))) float f32x4;
typedef __attribute__((ext_vector_type(16))) float f32x16;
typedef __attribute__((ext_vector_type(4))) unsigned u32x4;
typedef __attribute__((address_space(3))) bf16x4* lds_bf16x4_p;

constexpr int NBATCH = 8, NH = 32, NHKV = 8, DH = 128, PAGE = 16, NBLK = 512;
constexpr int NSLOT = 32;
constexpr int NTHR = NW * 64;
constexpr int CPT = NBLK / NTHR;
constexpr int MAXU = NBATCH * NBLK / NSLOT;
constexpr int PAGE_BYTES = PAGE * NHKV * DH * 2;
constexpr int TOK_BYTES = NHKV * DH * 2;
constexpr float SCALE_LOG2E = 0.08838834764831845f * 1.4426950408889634f;

constexpr int RING_BYTES = NW * DEPTH * 16384;
constexpr int OROW = 528;
constexpr int OBUF_BYTES = NW * 32 * OROW;
constexpr int BIG_BYTES = RING_BYTES > OBUF_BYTES ? RING_BYTES : OBUF_BYTES;
constexpr int L_CNT = BIG_BYTES;
constexpr int L_LIST = L_CNT + 64 * 4;
constexpr int L_M = L_LIST + MAXU * 8;
constexpr int L_L = L_M + NW * 32 * 4;
constexpr int LDS_BYTES = L_L + NW * 32 * 4;
static_assert(LDS_BYTES <= 163840, "lds");

constexpr size_t WS_ML = 0;
constexpr size_t WS_PO = (size_t)NHKV * 32 * NSLOT * 8;

__device__ __forceinline__ float swap32_max(float x) {
  auto r = __builtin_amdgcn_permlane32_swap(__float_as_uint(x), __float_as_uint(x), false, false);
  return fmaxf(__uint_as_float(r[0]), __uint_as_float(r[1]));
}
__device__ __forceinline__ float swap32_sum(float x) {
  auto r = __builtin_amdgcn_permlane32_swap(__float_as_uint(x), __float_as_uint(x), false, false);
  return __uint_as_float(r[0]) + __uint_as_float(r[1]);
}

#define K_POL " nt"
#define V_POL " nt"
#define DMA_PAGE_FN(NAME, POL) \
__device__ __forceinline__ void NAME(unsigned ldsaddr, u32x4 srd, unsigned soff, unsigned v0, unsigned v1, unsigned v2, unsigned v3) { \
  asm volatile( \
      "s_nop 4\n\t" \
      "s_mov_b32 m0, %0\n\t" \
      "s_nop 0\n\t" \
      "buffer_load_dwordx4 %3, %1, %2 offen" POL " lds\n\t" \
      "buffer_load_dwordx4 %4, %1, %2 offen offset:1024" POL " lds\n\t" \
      "buffer_load_dwordx4 %5, %1, %2 offen offset:2048" POL " lds\n\t" \
      "buffer_load_dwordx4 %6, %1, %2 offen offset:3072" POL " lds" \
      :: "s"(ldsaddr), "s"(srd), "s"(soff), "v"(v0), "v"(v1), "v"(v2), "v"(v3) : "memory"); \
}
DMA_PAGE_FN(dma_pageK, K_POL)
DMA_PAGE_FN(dma_pageV, V_POL)
#define WAITVM(n) asm volatile("s_waitcnt vmcnt(" #n ")" ::: "memory")
__device__ __forceinline__ void wait_vm4(int n4) {
  if (DEPTH == 1) {
    if (n4 >= 2) WAITVM(8); else if (n4 == 1) WAITVM(4); else WAITVM(0);
  } else if (DEPTH == 2) {
    if (n4 >= 6) WAITVM(24); else if (n4 == 5) WAITVM(20); else if (n4 == 4) WAITVM(16); else if (n4 == 3) WAITVM(12);
    else if (n4 == 2) WAITVM(8); else if (n4 == 1) WAITVM(4); else WAITVM(0);
  } else {
    if (n4 >= 14) WAITVM(56); else if (n4 >= 12) WAITVM(48); else if (n4 >= 10) WAITVM(40); else if (n4 >= 8) WAITVM(32);
    else if (n4 >= 6) WAITVM(24); else if (n4 == 5) WAITVM(20); else if (n4 == 4) WAITVM(16); else if (n4 == 3) WAITVM(12);
    else if (n4 == 2) WAITVM(8); else if (n4 == 1) WAITVM(4); else WAITVM(0);
  }
}

#define STAMP(k) do {} while (0)
__global__ void __launch_bounds__(NTHR, NW / 4)
attn_main(const __hip_bfloat16* __restrict__ q, const void* __restrict__ kc, const void* __restrict__ vc,
          const int* __restrict__ bt, const int* __restrict__ ctx, unsigned char* __restrict__ ws, unsigned kv_bytes) {
  __shared__ __attribute__((aligned(16))) char smem[LDS_BYTES];
  const int tid = threadIdx.x, lane = tid & 63;
  const int wave = __builtin_amdgcn_readfirstlane(tid >> 6);
  const int head = (blockIdx.x + HEAD_ROT) & 7, slot = blockIdx.x >> 3;
  const int col = lane & 31, hh = lane >> 5;
  STAMP(0);

  int id[CPT][NBATCH];
#pragma unroll
  for (int c = 0; c < CPT; ++c)
#pragma unroll
    for (int b = 0; b < NBATCH; ++b) id[c][b] = bt[b * NBLK + (wave * CPT + c) * 64 + lane];
  int cl[NBATCH];
#pragma unroll
  for (int b = 0; b < NBATCH; ++b) cl[b] = ctx[b];
  bf16x8 qf[8];
  {
    const __hip_bfloat16* qp = q + ((col >> 2) * NH + head * 4 + (col & 3)) * DH + 8 * hh;
#pragma unroll
    for (int s = 0; s < 8; ++s) qf[s] = *reinterpret_cast<const bf16x8*>(qp + 16 * s);
  }
  int myc = 0; bool myzero = false;
#pragma unroll
  for (int b = 0; b < NBATCH; ++b) {
    const bool z = cl[b] <= 0;
    cl[b] = z ? NBLK * PAGE : min(cl[b], NBLK * PAGE);
    if ((col >> 2) == b) { myc = cl[b]; myzero = z; }
  }
  unsigned msk[CPT][NBATCH];
  unsigned long long bal[CPT][NBATCH];
  int* cnt = reinterpret_cast<int*>(smem + L_CNT);
#pragma unroll
  for (int c = 0; c < CPT; ++c) {
    const int j = (wave * CPT + c) * 64 + lane;
#pragma unroll
    for (int b = 0; b < NBATCH; ++b) if (!(j * PAGE < cl[b])) id[c][b] = -1 - b;
#pragma unroll
    for (int b = 0; b < NBATCH; ++b) msk[c][b] = 1u << b;
#pragma unroll
    for (int b = 0; b < NBATCH; ++b)
#pragma unroll
      for (int e = b + 1; e < NBATCH; ++e) {
        const bool eq = id[c][b] == id[c][e];
        msk[c][b] |= eq ? (1u << e) : 0u;
        msk[c][e] |= eq ? (1u << b) : 0u;
      }
#pragma unroll
    for (int b = 0; b < NBATCH; ++b) {
      const bool lead = (j * PAGE < cl[b]) && (msk[c][b] & ((1u << b) - 1u)) == 0u;
      bal[c][b] = __ballot(lead);
      if (!lead) msk[c][b] = 0u;
    }
    int v = 0;
#pragma unroll
    for (int b = 0; b < NBATCH; ++b) if (lane == b) v = __popcll(bal[c][b]);
    if (lane < NBATCH) cnt[lane * 8 + wave * CPT + c] = v;
  }
  STAMP(1);
  __syncthreads();
  int lo, n;
  {
    const int own = cnt[lane];
    int incl = own;
#pragma unroll
    for (int o = 1; o < 64; o <<= 1) { const int t = __shfl_up(incl, o); if (lane >= o) incl += t; }
    const int excl = incl - own;
    const int U = __builtin_amdgcn_readlane(incl, 63);
    lo = (slot * U) / NSLOT;
    n = ((slot + 1) * U) / NSLOT - lo;
    uint2* list = reinterpret_cast<uint2*>(smem + L_LIST);
#pragma unroll
    for (int c = 0; c < CPT; ++c) {
      const int j = (wave * CPT + c) * 64 + lane;
#pragma unroll
      for (int b = 0; b < NBATCH; ++b) {
        const int base = __builtin_amdgcn_readlane(excl, b * 8 + wave * CPT + c) - lo;
        const int u = base + (int)__builtin_amdgcn_mbcnt_hi((unsigned)(bal[c][b] >> 32), __builtin_amdgcn_mbcnt_lo((unsigned)bal[c][b], 0u));
        if (msk[c][b] != 0u && (unsigned)u < (unsigned)n) list[u] = make_uint2((unsigned)id[c][b], msk[c][b] | ((unsigned)j << 8));
      }
    }
  }
  __syncthreads();
  const int ntiles = (n + 1) >> 1;
  const int nt = (ntiles > wave) ? (ntiles - wave + NW - 1) / NW : 0;
  const bool lastodd = (n & 1) && (nt > 0) && (wave + NW * (nt - 1) == ntiles - 1);
  uint4 ent = make_uint4(0u, 0u, 0u, 0u);
  if (lane < nt) ent = *reinterpret_cast<const uint4*>(smem + L_LIST + 16 * (wave + NW * lane));
  if (lastodd && lane == nt - 1) { ent.z = 0u; ent.w = 0u; }
  if (myzero) {
#pragma unroll
    for (int s = 0; s < 8; ++s) qf[s] = bf16x8{};
  }
#pragma unroll
  for (int s = 0; s < 8; ++s) asm volatile("" : "+v"(qf[s]));

  const uint64_t kb = (uint64_t)kc, vb = (uint64_t)vc;
  const u32x4 srdK = {(unsigned)kb, (unsigned)(kb >> 32) & 0xffffu, kv_bytes, 0x00020000u};
  const u32x4 srdV = {(unsigned)vb, (unsigned)(vb >> 32) & 0xffffu, kv_bytes, 0x00020000u};
  const unsigned g4 = lane >> 4, p16 = lane & 15;
  unsigned vk[4], vv[4];
#pragma unroll
  for (int i = 0; i < 4; ++i) {
    vk[i] = (4 * i + g4) * TOK_BYTES + 16 * (p16 ^ (4 * i + g4)) - 1024 * i;
    vv[i] = (4 * i + g4) * TOK_BYTES + 16 * (p16 ^ (g4 << 2)) - 1024 * i;
  }
  const unsigned sbase = (unsigned)(size_t)smem;
  const unsigned ring = sbase + wave * (DEPTH * 16384);
  const unsigned hoff = head * (DH * 2);

  auto issueK = [&](int i, int slotd) {
    const unsigned pa = __builtin_amdgcn_readlane(ent.x, i) * PAGE_BYTES + hoff;
    dma_pageK(ring + slotd * 16384, srdK, pa, vk[0], vk[1], vk[2], vk[3]);
    if (!(lastodd && i == nt - 1)) {
      const unsigned pb = __builtin_amdgcn_readlane(ent.z, i) * PAGE_BYTES + hoff;
      dma_pageK(ring + slotd * 16384 + 4096, srdK, pb, vk[0], vk[1], vk[2], vk[3]);
    }
  };
  auto issueV = [&](int i, int slotd) {
    const unsigned pa = __builtin_amdgcn_readlane(ent.x, i) * PAGE_BYTES + hoff;
    dma_pageV(ring + slotd * 16384 + 8192, srdV, pa, vv[0], vv[1], vv[2], vv[3]);
    if (!(lastodd && i == nt - 1)) {
      const unsigned pb = __builtin_amdgcn_readlane(ent.z, i) * PAGE_BYTES + hoff;
      dma_pageV(ring + slotd * 16384 + 12288, srdV, pb, vv[0], vv[1], vv[2], vv[3]);
    }
  };
  STAMP(2);
#pragma unroll
  for (int d = 0; d < DEPTH; ++d) if (d < nt) { issueK(d, d); issueV(d, d); }

  const unsigned krow = col * 256;
  const unsigned tq = (lane & 15) >> 2, tp = lane & 3, blk = (lane >> 4) & 1;
  const unsigned vrow = 8192 + (4 * hh + tq) * 256 + 8 * (tp & 1);

  f32x16 acc[4];
#pragma unroll
  for (int c = 0; c < 4; ++c) acc[c] = f32x16{};
  float m = -INFINITY, lsum = 0.f;
  const int tot4 = 2 * nt - (lastodd ? 1 : 0);

  int sd = 0;
  for (int i = 0; i < nt; ++i) {
    char* rb = smem + wave * (DEPTH * 16384) + sd * 16384;
    const int ahead = min(tot4, 2 * (i + DEPTH)) - min(tot4, 2 * (i + 1));
    const int npi = min(tot4 - 2 * i, 2);
    const int npn = max(0, min(tot4 - 2 * (i + DEPTH), 2));
    wait_vm4(npi + 2 * ahead);
    if (i == 0) STAMP(3);
    if (i == 1) STAMP(6);
    bf16x8 kf[8];
#pragma unroll
    for (int s = 0; s < 8; ++s) kf[s] = *reinterpret_cast<const bf16x8*>(rb + krow + 16 * ((2 * s + hh) ^ (col & 15)));
    asm volatile("s_waitcnt lgkmcnt(0)" ::: "memory");
    if (i + DEPTH < nt) issueK(i + DEPTH, sd);
    f32x16 sc = f32x16{};
#pragma unroll
    for (int s = 0; s < 8; ++s) sc = __builtin_amdgcn_mfma_f32_32x32x16_bf16(kf[s], qf[s], sc, 0, 0, 0);
    const unsigned ea = __builtin_amdgcn_readlane(ent.y, i), eb = __builtin_amdgcn_readlane(ent.w, i);
    const int nva = (((ea >> (col >> 2)) & 1u) ? myc - (int)(ea >> 8) * PAGE : 0) - 4 * hh;
    const int nvb = (((eb >> (col >> 2)) & 1u) ? myc - (int)(eb >> 8) * PAGE : 0) - 4 * hh;
    float mx = -INFINITY;
#pragma unroll
    for (int r = 0; r < 16; ++r) {
      const int t = (r & 3) + 8 * ((r >> 2) & 1);
      sc[r] = (t < (r < 8 ? nva : nvb)) ? sc[r] : -INFINITY;
      mx = fmaxf(mx, sc[r]);
    }
    mx = swap32_max(mx);
    const float mn = fmaxf(m, mx);
    const float msub = (mn == -INFINITY) ? 0.f : mn * SCALE_LOG2E;
    const float alpha = __builtin_amdgcn_exp2f(m * SCALE_LOG2E - msub);
    m = mn;
    float ps = 0.f;
#pragma unroll
    for (int r = 0; r < 16; ++r) { sc[r] = __builtin_amdgcn_exp2f(fmaf(sc[r], SCALE_LOG2E, -msub)); ps += sc[r]; }
    lsum = fmaf(lsum, alpha, ps);
    bf16x8 pb[2];
#pragma unroll
    for (int s = 0; s < 2; ++s)
#pragma unroll
      for (int k = 0; k < 4; ++k) {
        const bf16x2 t = __builtin_convertvector((f32x2{sc[8 * s + 2 * k], sc[8 * s + 2 * k + 1]}), bf16x2);
        pb[s][2 * k] = t[0]; pb[s][2 * k + 1] = t[1];
      }
#pragma unroll
    for (int c = 0; c < 4; ++c) acc[c] *= alpha;
    wait_vm4(2 * ahead + npn);
    bf16x8 vf[2][4];
#pragma unroll
    for (int s = 0; s < 2; ++s)
#pragma unroll
      for (int c = 0; c < 4; ++c) {
        char* a = rb + vrow + 16 * ((4 * c + 2 * blk + (tp >> 1)) ^ (tq << 2)) + s * 4096;
        const bf16x4 x = __builtin_amdgcn_ds_read_tr16_b64_v4bf16((lds_bf16x4_p)(a));
        const bf16x4 y = __builtin_amdgcn_ds_read_tr16_b64_v4bf16((lds_bf16x4_p)(a + 2048));
        vf[s][c] = __builtin_shufflevector(x, y, 0, 1, 2, 3, 4, 5, 6, 7);
      }
    asm volatile("s_waitcnt lgkmcnt(0)" ::: "memory");
    if (i + DEPTH < nt) issueV(i + DEPTH, sd);
#pragma unroll
    for (int c = 0; c < 4; ++c) acc[c] = __builtin_amdgcn_mfma_f32_32x32x16_bf16(vf[0][c], pb[0], acc[c], 0, 0, 0);
    if (npi == 2) {
#pragma unroll
      for (int c = 0; c < 4; ++c) acc[c] = __builtin_amdgcn_mfma_f32_32x32x16_bf16(vf[1][c], pb[1], acc[c], 0, 0, 0);
    }
    sd = (sd + 1 == DEPTH) ? 0 : sd + 1;
  }

  STAMP(4);
  float* sm = reinterpret_cast<float*>(smem + L_M);
  float* sl = reinterpret_cast<float*>(smem + L_L);
  const float ltot = swap32_sum(lsum);
  const float ms = m * SCALE_LOG2E;
  if (lane < 32) { sm[wave * 32 + lane] = ms; sl[wave * 32 + lane] = ltot; }
  __syncthreads();
  {
    float M = -INFINITY;
#pragma unroll
    for (int w = 0; w < NW; ++w) M = fmaxf(M, sm[w * 32 + col]);
    if (M != -INFINITY) {
      const float f = __builtin_amdgcn_exp2f(ms - M);
      char* ob = smem + (wave * 32 + col) * OROW + 16 * hh;
#pragma unroll
      for (int c = 0; c < 4; ++c)
#pragma unroll
        for (int g = 0; g < 4; ++g)
          *reinterpret_cast<f32x4*>(ob + 128 * c + 32 * g) = f32x4{acc[c][4 * g] * f, acc[c][4 * g + 1] * f, acc[c][4 * g + 2] * f, acc[c][4 * g + 3] * f};
    }
    if (tid < 32) {
      float L = 0.f;
      const float Ms = (M == -INFINITY) ? 0.f : M;
#pragma unroll
      for (int w = 0; w < NW; ++w) L = fmaf(sl[w * 32 + col], __builtin_amdgcn_exp2f(sm[w * 32 + col] - Ms), L);
      reinterpret_cast<float2*>(ws + WS_ML)[(head * 32 + col) * NSLOT + slot] = make_float2(M, L);
    }
  }
  __syncthreads();
#pragma unroll
  for (int it = 0; it < 1024 / NTHR; ++it) {
    const int cc = (tid >> 5) + it * (NTHR / 32), d4 = tid & 31;
    float M = -INFINITY;
#pragma unroll
    for (int w = 0; w < NW; ++w) M = fmaxf(M, sm[w * 32 + cc]);
    if (M != -INFINITY) {
      f32x4 o = f32x4{};
#pragma unroll
      for (int w = 0; w < NW; ++w) o += *reinterpret_cast<const f32x4*>(smem + (w * 32 + cc) * OROW + 16 * d4);
      *reinterpret_cast<f32x4*>(ws + WS_PO + ((size_t)((head * 32 + cc) * NSLOT + slot) * DH + 4 * d4) * 4) = o;
    }
  }
}

__global__ void __launch_bounds__(256) attn_combine(const unsigned char* __restrict__ ws, __hip_bfloat16* __restrict__ out) {
  __shared__ __attribute__((aligned(16))) float red[8][DH];
  const int head = blockIdx.x & 7, col = blockIdx.x >> 3;
  const int tid = threadIdx.x, s = tid & 31, grp = tid >> 5;
  const int row = head * 32 + col;
  const float2 ml = reinterpret_cast<const float2*>(ws + WS_ML)[row * NSLOT + s];
  float M = ml.x;
#pragma unroll
  for (int o = 16; o >= 1; o >>= 1) M = fmaxf(M, __shfl_xor(M, o));
  const float w = (ml.x == -INFINITY) ? 0.f : __builtin_amdgcn_exp2f(ml.x - M);
  float L = w * ml.y;
#pragma unroll
  for (int o = 16; o >= 1; o >>= 1) L += __shfl_xor(L, o);
  f32x4 part[4]; float wk[4];
#pragma unroll
  for (int k = 0; k < 4; ++k) {
    const int sl = grp + 8 * k;
    const float mk = __shfl(ml.x, (tid & 32) + sl);
    wk[k] = __shfl(w, (tid & 32) + sl);
    part[k] = f32x4{};
    if (mk != -INFINITY) part[k] = *reinterpret_cast<const f32x4*>(ws + WS_PO + ((size_t)(row * NSLOT + sl) * DH + 4 * s) * 4);
  }
  f32x4 o = f32x4{};
#pragma unroll
  for (int k = 0; k < 4; ++k) o += part[k] * wk[k];
  *reinterpret_cast<f32x4*>(&red[grp][4 * s]) = o;
  __syncthreads();
  if (tid < 64) {
    float a = 0.f, b = 0.f;
#pragma unroll
    for (int g = 0; g < 8; ++g) { a += red[g][2 * tid]; b += red[g][2 * tid + 1]; }
    const float inv = 1.f / L;
    const bf16x2 r = __builtin_convertvector((f32x2{a * inv, b * inv}), bf16x2);
    *reinterpret_cast<bf16x2*>(out + ((col >> 2) * NH + head * 4 + (col & 3)) * DH + 2 * tid) = r;
  }
}

extern "C" void kernel_launch(void* const* d_in, const int* in_sizes, int n_in, void* d_out, int out_size, void* d_ws, size_t ws_size,
                              hipStream_t stream) {
  const __hip_bfloat16* q = (const __hip_bfloat16*)d_in[0];
  const unsigned kv_bytes = (unsigned)in_sizes[1] * 2u;
  attn_main<<<NHKV * NSLOT, NTHR, 0, stream>>>(q, d_in[1], d_in[2], (const int*)d_in[3], (const int*)d_in[4], (unsigned char*)d_ws, kv_bytes);
  attn_combine<<<NHKV * 32, 256, 0, stream>>>((const unsigned char*)d_ws, (__hip_bfloat16*)d_out);
}
```

```cpp
#include <hip/hip_runtime.h>
#include <hip/hip_bf16.h>
#include <stdint.h>

#define STAMPS
#define LEAK 0
#define NW 8
#define DEPTH 1
#define HEAD_ROT 0
#define HEAD_MAJOR 1

typedef __attribute__((ext_vector_type(8))) __bf16 bf16x8;
typedef __attribute__((ext_vector_type(4))) __bf16 bf16x4;
typedef __attribute__((ext_vector_type(2))) __bf16 bf16x2;
typedef __attribute__((ext_vector_type(2))) float f32x2;
typedef __attribute__((ext_vector_type(4))) float f32x4;
typedef __attribute__((ext_vector_type(16))) float f32x16;
typedef __attribute__((ext_vector_type(4))) unsigned u32x4;
typedef __attribute__((address_space(3))) bf16x4* lds_bf16x4_p;

constexpr int NBATCH = 8, NH = 32, NHKV = 8, DH = 128, PAGE = 16, NBLK = 512;
constexpr int NSLOT = 32;
constexpr int NTHR = NW * 64;
constexpr int CPT = NBLK / NTHR;
constexpr int MAXU = NBATCH * NBLK / NSLOT;
constexpr int PAGE_BYTES = PAGE * NHKV * DH * 2;
constexpr int TOK_BYTES = NHKV * DH * 2;
constexpr float SCALE_LOG2E = 0.08838834764831845f * 1.4426950408889634f;

constexpr int RING_BYTES = NW * DEPTH * 16384;
constexpr int OROW = 528;
constexpr int OBUF_BYTES = NW * 32 * OROW;
constexpr int BIG_BYTES = RING_BYTES > OBUF_BYTES ? RING_BYTES : OBUF_BYTES;
constexpr int L_CNT = BIG_BYTES;
constexpr int L_LIST = L_CNT + 64 * 4;
constexpr int L_M = L_LIST + MAXU * 8;
constexpr int L_L = L_M + NW * 32 * 4;
constexpr int LDS_BYTES = L_L + NW * 32 * 4;
static_assert(LDS_BYTES <= 163840, "lds");

constexpr size_t WS_ML = 0;
constexpr size_t WS_PO = (size_t)NHKV * 32 * NSLOT * 8;

__device__ __forceinline__ float swap32_max(float x) {
  auto r = __builtin_amdgcn_permlane32_swap(__float_as_uint(x), __float_as_uint(x), false, false);
  return fmaxf(__uint_as_float(r[0]), __uint_as_float(r[1]));
}
__device__ __forceinline__ float swap32_sum(float x) {
  auto r = __builtin_amdgcn_permlane32_swap(__float_as_uint(x), __float_as_uint(x), false, false);
  return __uint_as_float(r[0]) + __uint_as_float(r[1]);
}

#define K_POL " nt"
#define V_POL " nt"
#define DMA_PAGE_FN(NAME, POL) \
__device__ __forceinline__ void NAME(unsigned ldsaddr, u32x4 srd, unsigned soff, unsigned v0, unsigned v1, unsigned v2, unsigned v3) { \
  asm volatile( \
      "s_nop 4\n\t" \
      "s_mov_b32 m0, %0\n\t" \
      "s_nop 0\n\t" \
      "buffer_load_dwordx4 %3, %1, %2 offen" POL " lds\n\t" \
      "buffer_load_dwordx4 %4, %1, %2 offen offset:1024" POL " lds\n\t" \
      "buffer_load_dwordx4 %5, %1, %2 offen offset:2048" POL " lds\n\t" \
      "buffer_load_dwordx4 %6, %1, %2 offen offset:3072" POL " lds" \
      :: "s"(ldsaddr), "s"(srd), "s"(soff), "v"(v0), "v"(v1), "v"(v2), "v"(v3) : "memory"); \
}
DMA_PAGE_FN(dma_pageK, K_POL)
DMA_PAGE_FN(dma_pageV, V_POL)
#define WAITVM(n) asm volatile("s_waitcnt vmcnt(" #n ")" ::: "memory")
__device__ __forceinline__ void wait_vm4(int n4) {
  if (DEPTH == 1) {
    if (n4 >= 2) WAITVM(8); else if (n4 == 1) WAITVM(4); else WAITVM(0);
  } else if (DEPTH == 2) {
    if (n4 >= 6) WAITVM(24); else if (n4 == 5) WAITVM(20); else if (n4 == 4) WAITVM(16); else if (n4 == 3) WAITVM(12);
    else if (n4 == 2) WAITVM(8); else if (n4 == 1) WAITVM(4); else WAITVM(0);
  } else {
    if (n4 >= 14) WAITVM(56); else if (n4 >= 12) WAITVM(48); else if (n4 >= 10) WAITVM(40); else if (n4 >= 8) WAITVM(32);
    else if (n4 >= 6) WAITVM(24); else if (n4 == 5) WAITVM(20); else if (n4 == 4) WAITVM(16); else if (n4 == 3) WAITVM(12);
    else if (n4 == 2) WAITVM(8); else if (n4 == 1) WAITVM(4); else WAITVM(0);
  }
}

constexpr size_t WS_STAMP = (size_t)64 << 20;
#define STAMP(k) do { st_[k] = __builtin_amdgcn_s_memrealtime(); } while (0)
__global__ void __launch_bounds__(NTHR, NW / 4)
attn_main(const __hip_bfloat16* __restrict__ q, const void* __restrict__ kc, const void* __restrict__ vc,
          const int* __restrict__ bt, const int* __restrict__ ctx, unsigned char* __restrict__ ws, unsigned kv_bytes) {
  __shared__ __attribute__((aligned(16))) char smem[LDS_BYTES];
  const int tid = threadIdx.x, lane = tid & 63;
  const int wave = __builtin_amdgcn_readfirstlane(tid >> 6);
  const int head = (blockIdx.x + HEAD_ROT) & 7, slot = blockIdx.x >> 3;
  const int col = lane & 31, hh = lane >> 5;
  unsigned long long st_[8] = {0, 0, 0, 0, 0, 0, 0, 0};
  STAMP(0);

  int id[CPT][NBATCH];
#pragma unroll
  for (int c = 0; c < CPT; ++c)
#pragma unroll
    for (int b = 0; b < NBATCH; ++b) id[c][b] = bt[b * NBLK + (wave * CPT + c) * 64 + lane];
  int cl[NBATCH];
#pragma unroll
  for (int b = 0; b < NBATCH; ++b) cl[b] = ctx[b];
  bf16x8 qf[8];
  {
    const __hip_bfloat16* qp = q + ((col >> 2) * NH + head * 4 + (col & 3)) * DH + 8 * hh;
#pragma unroll
    for (int s = 0; s < 8; ++s) qf[s] = *reinterpret_cast<const bf16x8*>(qp + 16 * s);
  }
  int myc = 0; bool myzero = false;
#pragma unroll
  for (int b = 0; b < NBATCH; ++b) {
    const bool z = cl[b] <= 0;
    cl[b] = z ? NBLK * PAGE : min(cl[b], NBLK * PAGE);
    if ((col >> 2) == b) { myc = cl[b]; myzero = z; }
  }
  unsigned msk[CPT][NBATCH];
  unsigned long long bal[CPT][NBATCH];
  int* cnt = reinterpret_cast<int*>(smem + L_CNT);
#pragma unroll
  for (int c = 0; c < CPT; ++c) {
    const int j = (wave * CPT + c) * 64 + lane;
#pragma unroll
    for (int b = 0; b < NBATCH; ++b) if (!(j * PAGE < cl[b])) id[c][b] = -1 - b;
#pragma unroll
    for (int b = 0; b < NBATCH; ++b) msk[c][b] = 1u << b;
#pragma unroll
    for (int b = 0; b < NBATCH; ++b)
#pragma unroll
      for (int e = b + 1; e < NBATCH; ++e) {
        const bool eq = id[c][b] == id[c][e];
        msk[c][b] |= eq ? (1u << e) : 0u;
        msk[c][e] |= eq ? (1u << b) : 0u;
      }
#pragma unroll
    for (int b = 0; b < NBATCH; ++b) {
      const bool lead = (j * PAGE < cl[b]) && (msk[c][b] & ((1u << b) - 1u)) == 0u;
      bal[c][b] = __ballot(lead);
      if (!lead) msk[c][b] = 0u;
    }
    int v = 0;
#pragma unroll
    for (int b = 0; b < NBATCH; ++b) if (lane == b) v = __popcll(bal[c][b]);
    if (lane < NBATCH) cnt[lane * 8 + wave * CPT + c] = v;
  }
  STAMP(1);
  __syncthreads();
  int lo, n;
  {
    const int own = cnt[lane];
    int incl = own;
#pragma unroll
    for (int o = 1; o < 64; o <<= 1) { const int t = __shfl_up(incl, o); if (lane >= o) incl += t; }
    const int excl = incl - own;
    const int U = __builtin_amdgcn_readlane(incl, 63);
    lo = (slot * U) / NSLOT;
    n = ((slot + 1) * U) / NSLOT - lo;
    uint2* list = reinterpret_cast<uint2*>(smem + L_LIST);
#pragma unroll
    for (int c = 0; c < CPT; ++c) {
      const int j = (wave * CPT + c) * 64 + lane;
#pragma unroll
      for (int b = 0; b < NBATCH; ++b) {
        const int base = __builtin_amdgcn_readlane(excl, b * 8 + wave * CPT + c) - lo;
        const int u = base + (int)__builtin_amdgcn_mbcnt_hi((unsigned)(bal[c][b] >> 32), __builtin_amdgcn_mbcnt_lo((unsigned)bal[c][b], 0u));
        if (msk[c][b] != 0u && (unsigned)u < (unsigned)n) list[u] = make_uint2((unsigned)id[c][b], msk[c][b] | ((unsigned)j << 8));
      }
    }
  }
  __syncthreads();
  const int ntiles = (n + 1) >> 1;
  const int nt = (ntiles > wave) ? (ntiles - wave + NW - 1) / NW : 0;
  const bool lastodd = (n & 1) && (nt > 0) && (wave + NW * (nt - 1) == ntiles - 1);
  uint4 ent = make_uint4(0u, 0u, 0u, 0u);
  if (lane < nt) ent = *reinterpret_cast<const uint4*>(smem + L_LIST + 16 * (wave + NW * lane));
  if (lastodd && lane == nt - 1) { ent.z = 0u; ent.w = 0u; }
  if (myzero) {
#pragma unroll
    for (int s = 0; s < 8; ++s) qf[s] = bf16x8{};
  }
#pragma unroll
  for (int s = 0; s < 8; ++s) asm volatile("" : "+v"(qf[s]));

  const uint64_t kb = (uint64_t)kc, vb = (uint64_t)vc;
  const u32x4 srdK = {(unsigned)kb, (unsigned)(kb >> 32) & 0xffffu, kv_bytes, 0x00020000u};
  const u32x4 srdV = {(unsigned)vb, (unsigned)(vb >> 32) & 0xffffu, kv_bytes, 0x00020000u};
  const unsigned g4 = lane >> 4, p16 = lane & 15;
  unsigned vk[4], vv[4];
#pragma unroll
  for (int i = 0; i < 4; ++i) {
    vk[i] = (4 * i + g4) * TOK_BYTES + 16 * (p16 ^ (4 * i + g4)) - 1024 * i;
    vv[i] = (4 * i + g4) * TOK_BYTES + 16 * (p16 ^ (g4 << 2)) - 1024 * i;
  }
  const unsigned sbase = (unsigned)(size_t)smem;
  const unsigned ring = sbase + wave * (DEPTH * 16384);
  const unsigned hoff = head * (DH * 2);

  auto issueK = [&](int i, int slotd) {
    const unsigned pa = __builtin_amdgcn_readlane(ent.x, i) * PAGE_BYTES + hoff;
    dma_pageK(ring + slotd * 16384, srdK, pa, vk[0], vk[1], vk[2], vk[3]);
    if (!(lastodd && i == nt - 1)) {
      const unsigned pb = __builtin_amdgcn_readlane(ent.z, i) * PAGE_BYTES + hoff;
      dma_pageK(ring + slotd * 16384 + 4096, srdK, pb, vk[0], vk[1], vk[2], vk[3]);
    }
  };
  auto issueV = [&](int i, int slotd) {
    const unsigned pa = __builtin_amdgcn_readlane(ent.x, i) * PAGE_BYTES + hoff;
    dma_pageV(ring + slotd * 16384 + 8192, srdV, pa, vv[0], vv[1], vv[2], vv[3]);
    if (!(lastodd && i == nt - 1)) {
      const unsigned pb = __builtin_amdgcn_readlane(ent.z, i) * PAGE_BYTES + hoff;
      dma_pageV(ring + slotd * 16384 + 12288, srdV, pb, vv[0], vv[1], vv[2], vv[3]);
    }
  };
  STAMP(2);
#pragma unroll
  for (int d = 0; d < DEPTH; ++d) if (d < nt) { issueK(d, d); issueV(d, d); }

  const unsigned krow = col * 256;
  const unsigned tq = (lane & 15) >> 2, tp = lane & 3, blk = (lane >> 4) & 1;
  const unsigned vrow = 8192 + (4 * hh + tq) * 256 + 8 * (tp & 1);

  f32x16 acc[4];
#pragma unroll
  for (int c = 0; c < 4; ++c) acc[c] = f32x16{};
  float m = -INFINITY, lsum = 0.f;
  const int tot4 = 2 * nt - (lastodd ? 1 : 0);

  int sd = 0;
  for (int i = 0; i < nt; ++i) {
    char* rb = smem + wave * (DEPTH * 16384) + sd * 16384;
    const int ahead = min(tot4, 2 * (i + DEPTH)) - min(tot4, 2 * (i + 1));
    const int npi = min(tot4 - 2 * i, 2);
    const int npn = max(0, min(tot4 - 2 * (i + DEPTH), 2));
    wait_vm4(npi + 2 * ahead);
    if (i == 0) STAMP(3);
    if (i == 1) STAMP(6);
    bf16x8 kf[8];
#pragma unroll
    for (int s = 0; s < 8; ++s) kf[s] = *reinterpret_cast<const bf16x8*>(rb + krow + 16 * ((2 * s + hh) ^ (col & 15)));
    asm volatile("s_waitcnt lgkmcnt(0)" ::: "memory");
    if (i + DEPTH < nt) issueK(i + DEPTH, sd);
    f32x16 sc = f32x16{};
#pragma unroll
    for (int s = 0; s < 8; ++s) sc = __builtin_amdgcn_mfma_f32_32x32x16_bf16(kf[s], qf[s], sc, 0, 0, 0);
    const unsigned ea = __builtin_amdgcn_readlane(ent.y, i), eb = __builtin_amdgcn_readlane(ent.w, i);
    const int nva = (((ea >> (col >> 2)) & 1u) ? myc - (int)(ea >> 8) * PAGE : 0) - 4 * hh;
    const int nvb = (((eb >> (col >> 2)) & 1u) ? myc - (int)(eb >> 8) * PAGE : 0) - 4 * hh;
    float mx = -INFINITY;
#pragma unroll
    for (int r = 0; r < 16; ++r) {
      const int t = (r & 3) + 8 * ((r >> 2) & 1);
      sc[r] = (t < (r < 8 ? nva : nvb)) ? sc[r] : -INFINITY;
      mx = fmaxf(mx, sc[r]);
    }
    mx = swap32_max(mx);
    const float mn = fmaxf(m, mx);
    const float msub = (mn == -INFINITY) ? 0.f : mn * SCALE_LOG2E;
    const float alpha = __builtin_amdgcn_exp2f(m * SCALE_LOG2E - msub);
    m = mn;
    float ps = 0.f;
#pragma unroll
    for (int r = 0; r < 16; ++r) { sc[r] = __builtin_amdgcn_exp2f(fmaf(sc[r], SCALE_LOG2E, -msub)); ps += sc[r]; }
    lsum = fmaf(lsum, alpha, ps);
    bf16x8 pb[2];
#pragma unroll
    for (int s = 0; s < 2; ++s)
#pragma unroll
      for (int k = 0; k < 4; ++k) {
        const bf16x2 t = __builtin_convertvector((f32x2{sc[8 * s + 2 * k], sc[8 * s + 2 * k + 1]}), bf16x2);
        pb[s][2 * k] = t[0]; pb[s][2 * k + 1] = t[1];
      }
#pragma unroll
    for (int c = 0; c < 4; ++c) acc[c] *= alpha;
    wait_vm4(2 * ahead + npn);
    bf16x8 vf[2][4];
#pragma unroll
    for (int s = 0; s < 2; ++s)
#pragma unroll
      for (int c = 0; c < 4; ++c) {
        char* a = rb + vrow + 16 * ((4 * c + 2 * blk + (tp >> 1)) ^ (tq << 2)) + s * 4096;
        const bf16x4 x = __builtin_amdgcn_ds_read_tr16_b64_v4bf16((lds_bf16x4_p)(a));
        const bf16x4 y = __builtin_amdgcn_ds_read_tr16_b64_v4bf16((lds_bf16x4_p)(a + 2048));
        vf[s][c] = __builtin_shufflevector(x, y, 0, 1, 2, 3, 4, 5, 6, 7);
      }
    asm volatile("s_waitcnt lgkmcnt(0)" ::: "memory");
    if (i + DEPTH < nt) issueV(i + DEPTH, sd);
#pragma unroll
    for (int c = 0; c < 4; ++c) acc[c] = __builtin_amdgcn_mfma_f32_32x32x16_bf16(vf[0][c], pb[0], acc[c], 0, 0, 0);
    if (npi == 2) {
#pragma unroll
      for (int c = 0; c < 4; ++c) acc[c] = __builtin_amdgcn_mfma_f32_32x32x16_bf16(vf[1][c], pb[1], acc[c], 0, 0, 0);
    }
    sd = (sd + 1 == DEPTH) ? 0 : sd + 1;
  }

  STAMP(4);
  float* sm = reinterpret_cast<float*>(smem + L_M);
  float* sl = reinterpret_cast<float*>(smem + L_L);
  const float ltot = swap32_sum(lsum);
  const float ms = m * SCALE_LOG2E;
  if (lane < 32) { sm[wave * 32 + lane] = ms; sl[wave * 32 + lane] = ltot; }
  __syncthreads();
  {
    float M = -INFINITY;
#pragma unroll
    for (int w = 0; w < NW; ++w) M = fmaxf(M, sm[w * 32 + col]);
    if (M != -INFINITY) {
      const float f = __builtin_amdgcn_exp2f(ms - M);
      char* ob = smem + (wave * 32 + col) * OROW + 16 * hh;
#pragma unroll
      for (int c = 0; c < 4; ++c)
#pragma unroll
        for (int g = 0; g < 4; ++g)
          *reinterpret_cast<f32x4*>(ob + 128 * c + 32 * g) = f32x4{acc[c][4 * g] * f, acc[c][4 * g + 1] * f, acc[c][4 * g + 2] * f, acc[c][4 * g + 3] * f};
    }
    if (tid < 32) {
      float L = 0.f;
      const float Ms = (M == -INFINITY) ? 0.f : M;
#pragma unroll
      for (int w = 0; w < NW; ++w) L = fmaf(sl[w * 32 + col], __builtin_amdgcn_exp2f(sm[w * 32 + col] - Ms), L);
      reinterpret_cast<float2*>(ws + WS_ML)[(head * 32 + col) * NSLOT + slot] = make_float2(M, L);
    }
  }
  __syncthreads();
#pragma unroll
  for (int it = 0; it < 1024 / NTHR; ++it) {
    const int cc = (tid >> 5) + it * (NTHR / 32), d4 = tid & 31;
    float M = -INFINITY;
#pragma unroll
    for (int w = 0; w < NW; ++w) M = fmaxf(M, sm[w * 32 + cc]);
    if (M != -INFINITY) {
      f32x4 o = f32x4{};
#pragma unroll
      for (int w = 0; w < NW; ++w) o += *reinterpret_cast<const f32x4*>(smem + (w * 32 + cc) * OROW + 16 * d4);
      *reinterpret_cast<f32x4*>(ws + WS_PO + ((size_t)((head * 32 + cc) * NSLOT + slot) * DH + 4 * d4) * 4) = o;
    }
  }
  asm volatile("s_waitcnt vmcnt(0)" ::: "memory");
  STAMP(5);
  st_[7] = nt;
  if (lane < 8) {
    unsigned long long v = 0;
#pragma unroll
    for (int k = 0; k < 8; ++k) if (lane == k) v = st_[k];
    reinterpret_cast<unsigned long long*>(ws + WS_STAMP)[(blockIdx.x * NW + wave) * 8 + lane] = v;
  }
}

__global__ void __launch_bounds__(256) attn_combine(const unsigned char* __restrict__ ws, __hip_bfloat16* __restrict__ out) {
  __shared__ __attribute__((aligned(16))) float red[8][DH];
  const unsigned long long tc0 = __builtin_amdgcn_s_memrealtime();
  const int head = blockIdx.x & 7, col = blockIdx.x >> 3;
  const int tid = threadIdx.x, s = tid & 31, grp = tid >> 5;
  const int row = head * 32 + col;
  const float2 ml = reinterpret_cast<const float2*>(ws + WS_ML)[row * NSLOT + s];
  float M = ml.x;
#pragma unroll
  for (int o = 16; o >= 1; o >>= 1) M = fmaxf(M, __shfl_xor(M, o));
  const float w = (ml.x == -INFINITY) ? 0.f : __builtin_amdgcn_exp2f(ml.x - M);
  float L = w * ml.y;
#pragma unroll
  for (int o = 16; o >= 1; o >>= 1) L += __shfl_xor(L, o);
  f32x4 part[4]; float wk[4];
#pragma unroll
  for (int k = 0; k < 4; ++k) {
    const int sl = grp + 8 * k;
    const float mk = __shfl(ml.x, (tid & 32) + sl);
    wk[k] = __shfl(w, (tid & 32) + sl);
    part[k] = f32x4{};
    if (mk != -INFINITY) part[k] = *reinterpret_cast<const f32x4*>(ws + WS_PO + ((size_t)(row * NSLOT + sl) * DH + 4 * s) * 4);
  }
  f32x4 o = f32x4{};
#pragma unroll
  for (int k = 0; k < 4; ++k) o += part[k] * wk[k];
  *reinterpret_cast<f32x4*>(&red[grp][4 * s]) = o;
  __syncthreads();
  if (tid < 64) {
    float a = 0.f, b = 0.f;
#pragma unroll
    for (int g = 0; g < 8; ++g) { a += red[g][2 * tid]; b += red[g][2 * tid + 1]; }
    const float inv = 1.f / L;
    const bf16x2 r = __builtin_convertvector((f32x2{a * inv, b * inv}), bf16x2);
    *reinterpret_cast<bf16x2*>(out + ((col >> 2) * NH + head * 4 + (col & 3)) * DH + 2 * tid) = r;
  }
  if (blockIdx.x == 0) {
    __shared__ unsigned long long mn[8], mx[8];
    if (tid < 8) { mn[tid] = ~0ull; mx[tid] = 0ull; }
    __syncthreads();
    const unsigned long long* sp = reinterpret_cast<const unsigned long long*>(ws + WS_STAMP) + (size_t)tid * NW * 8;
    for (int k = 0; k < 6; ++k) {
      unsigned long long a = ~0ull, b = 0ull;
      for (int w = 0; w < NW; ++w) { const unsigned long long v = sp[w * 8 + k]; a = v < a ? v : a; b = v > b ? v : b; }
      atomicMin(&mn[k], a); atomicMax(&mx[k], b);
    }
    __syncthreads();
    unsigned long long d = 0;
    if (LEAK == 1) d = mx[0] - mn[0];
    if (LEAK == 2) d = mx[5] - mn[0];
    if (LEAK == 3) d = mx[2] - mn[0];
    if (LEAK == 4) d = tc0 - mx[5];
    if (LEAK == 5) d = mn[2] - mn[0];
    if (LEAK == 6) d = mx[4] - mn[0];
    d = d > 10000ull ? 10000ull : d;
    const unsigned long long t1 = __builtin_amdgcn_s_memrealtime() + 4 * d;
    while (__builtin_amdgcn_s_memrealtime() < t1) __builtin_amdgcn_s_sleep(1);
  }
}

extern "C" void kernel_launch(void* const* d_in, const int* in_sizes, int n_in, void* d_out, int out_size, void* d_ws, size_t ws_size,
                              hipStream_t stream) {
  const __hip_bfloat16* q = (const __hip_bfloat16*)d_in[0];
  const unsigned kv_bytes = (unsigned)in_sizes[1] * 2u;
  attn_main<<<NHKV * NSLOT, NTHR, 0, stream>>>(q, d_in[1], d_in[2], (const int*)d_in[3], (const int*)d_in[4], (unsigned char*)d_ws, kv_bytes);
  attn_combine<<<NHKV * 32, 256, 0, stream>>>((const unsigned char*)d_ws, (__hip_bfloat16*)d_out);
}
```

```cpp
#include <hip/hip_runtime.h>
#include <hip/hip_bf16.h>
#include <stdint.h>

#define STAMPS
#define LEAK 0
#define NW 8
#define DEPTH 1
#define HEAD_ROT 0
#define HEAD_MAJOR 1

typedef __attribute__((ext_vector_type(8))) __bf16 bf16x8;
typedef __attribute__((ext_vector_type(4))) __bf16 bf16x4;
typedef __attribute__((ext_vector_type(2))) __bf16 bf16x2;
typedef __attribute__((ext_vector_type(2))) float f32x2;
typedef __attribute__((ext_vector_type(4))) float f32x4;
typedef __attribute__((ext_vector_type(16))) float f32x16;
typedef __attribute__((ext_vector_type(4))) unsigned u32x4;
typedef __attribute__((address_space(3))) bf16x4* lds_bf16x4_p;

constexpr int NBATCH = 8, NH = 32, NHKV = 8, DH = 128, PAGE = 16, NBLK = 512;
constexpr int NSLOT = 32;
constexpr int NTHR = NW * 64;
constexpr int CPT = NBLK / NTHR;
constexpr int MAXU = NBATCH * NBLK / NSLOT;
constexpr int PAGE_BYTES = PAGE * NHKV * DH * 2;
constexpr int TOK_BYTES = NHKV * DH * 2;
constexpr float SCALE_LOG2E = 0.08838834764831845f * 1.4426950408889634f;

constexpr int RING_BYTES = NW * DEPTH * 16384;
constexpr int OROW = 528;
constexpr int OBUF_BYTES = NW * 32 * OROW;
constexpr int BIG_BYTES = RING_BYTES > OBUF_BYTES ? RING_BYTES : OBUF_BYTES;
constexpr int L_CNT = BIG_BYTES;
constexpr int L_LIST = L_CNT + 64 * 4;
constexpr int L_M = L_LIST + MAXU * 8;
constexpr int L_L = L_M + NW * 32 * 4;
constexpr int LDS_BYTES = L_L + NW * 32 * 4;
static_assert(LDS_BYTES <= 163840, "lds");

constexpr size_t WS_ML = 0;
constexpr size_t WS_PO = (size_t)NHKV * 32 * NSLOT * 8;

__device__ __forceinline__ float swap32_max(float x) {
  auto r = __builtin_amdgcn_permlane32_swap(__float_as_uint(x), __float_as_uint(x), false, false);
  return fmaxf(__uint_as_float(r[0]), __uint_as_float(r[1]));
}
__device__ __forceinline__ float swap32_sum(float x) {
  auto r = __builtin_amdgcn_permlane32_swap(__float_as_uint(x), __float_as_uint(x), false, false);
  return __uint_as_float(r[0]) + __uint_as_float(r[1]);
}

#define K_POL " nt"
#define V_POL " nt"
#define DMA_PAGE_FN(NAME, POL) \
__device__ __forceinline__ void NAME(unsigned ldsaddr, u32x4 srd, unsigned soff, unsigned v0, unsigned v1, unsigned v2, unsigned v3) { \
  asm volatile( \
      "s_nop 4\n\t" \
      "s_mov_b32 m0, %0\n\t" \
      "s_nop 0\n\t" \
      "buffer_load_dwordx4 %3, %1, %2 offen" POL " lds\n\t" \
      "buffer_load_dwordx4 %4, %1, %2 offen offset:1024" POL " lds\n\t" \
      "buffer_load_dwordx4 %5, %1, %2 offen offset:2048" POL " lds\n\t" \
      "buffer_load_dwordx4 %6, %1, %2 offen offset:3072" POL " lds" \
      :: "s"(ldsaddr), "s"(srd), "s"(soff), "v"(v0), "v"(v1), "v"(v2), "v"(v3) : "memory"); \
}
DMA_PAGE_FN(dma_pageK, K_POL)
DMA_PAGE_FN(dma_pageV, V_POL)
#define WAITVM(n) asm volatile("s_waitcnt vmcnt(" #n ")" ::: "memory")
__device__ __forceinline__ void wait_vm4(int n4) {
  if (DEPTH == 1) {
    if (n4 >= 2) WAITVM(8); else if (n4 == 1) WAITVM(4); else WAITVM(0);
  } else if (DEPTH == 2) {
    if (n4 >= 6) WAITVM(24); else if (n4 == 5) WAITVM(20); else if (n4 == 4) WAITVM(16); else if (n4 == 3) WAITVM(12);
    else if (n4 == 2) WAITVM(8); else if (n4 == 1) WAITVM(4); else WAITVM(0);
  } else {
    if (n4 >= 14) WAITVM(56); else if (n4 >= 12) WAITVM(48); else if (n4 >= 10) WAITVM(40); else if (n4 >= 8) WAITVM(32);
    else if (n4 >= 6) WAITVM(24); else if (n4 == 5) WAITVM(20); else if (n4 == 4) WAITVM(16); else if (n4 == 3) WAITVM(12);
    else if (n4 == 2) WAITVM(8); else if (n4 == 1) WAITVM(4); else WAITVM(0);
  }
}

constexpr size_t WS_STAMP = (size_t)64 << 20;
#define STAMP(k) do { st_[k] = __builtin_amdgcn_s_memrealtime(); } while (0)
__global__ void __launch_bounds__(NTHR, NW / 4)
attn_main(const __hip_bfloat16* __restrict__ q, const void* __restrict__ kc, const void* __restrict__ vc,
          const int* __restrict__ bt, const int* __restrict__ ctx, unsigned char* __restrict__ ws, unsigned kv_bytes) {
  __shared__ __attribute__((aligned(16))) char smem[LDS_BYTES];
  const int tid = threadIdx.x, lane = tid & 63;
  const int wave = __builtin_amdgcn_readfirstlane(tid >> 6);
  const int head = (blockIdx.x + HEAD_ROT) & 7, slot = blockIdx.x >> 3;
  const int col = lane & 31, hh = lane >> 5;
  unsigned long long st_[8] = {0, 0, 0, 0, 0, 0, 0, 0};
  STAMP(0);

  int id[CPT][NBATCH];
#pragma unroll
  for (int c = 0; c < CPT; ++c)
#pragma unroll
    for (int b = 0; b < NBATCH; ++b) id[c][b] = bt[b * NBLK + (wave * CPT + c) * 64 + lane];
  int cl[NBATCH];
#pragma unroll
  for (int b = 0; b < NBATCH; ++b) cl[b] = ctx[b];
  bf16x8 qf[8];
  {
    const __hip_bfloat16* qp = q + ((col >> 2) * NH + head * 4 + (col & 3)) * DH + 8 * hh;
#pragma unroll
    for (int s = 0; s < 8; ++s) qf[s] = *reinterpret_cast<const bf16x8*>(qp + 16 * s);
  }
  int myc = 0; bool myzero = false;
#pragma unroll
  for (int b = 0; b < NBATCH; ++b) {
    const bool z = cl[b] <= 0;
    cl[b] = z ? NBLK * PAGE : min(cl[b], NBLK * PAGE);
    if ((col >> 2) == b) { myc = cl[b]; myzero = z; }
  }
  unsigned msk[CPT][NBATCH];
  unsigned long long bal[CPT][NBATCH];
  int* cnt = reinterpret_cast<int*>(smem + L_CNT);
#pragma unroll
  for (int c = 0; c < CPT; ++c) {
    const int j = (wave * CPT + c) * 64 + lane;
#pragma unroll
    for (int b = 0; b < NBATCH; ++b) if (!(j * PAGE < cl[b])) id[c][b] = -1 - b;
#pragma unroll
    for (int b = 0; b < NBATCH; ++b) msk[c][b] = 1u << b;
#pragma unroll
    for (int b = 0; b < NBATCH; ++b)
#pragma unroll
      for (int e = b + 1; e < NBATCH; ++e) {
        const bool eq = id[c][b] == id[c][e];
        msk[c][b] |= eq ? (1u << e) : 0u;
        msk[c][e] |= eq ? (1u << b) : 0u;
      }
#pragma unroll
    for (int b = 0; b < NBATCH; ++b) {
      const bool lead = (j * PAGE < cl[b]) && (msk[c][b] & ((1u << b) - 1u)) == 0u;
      bal[c][b] = __ballot(lead);
      if (!lead) msk[c][b] = 0u;
    }
    int v = 0;
#pragma unroll
    for (int b = 0; b < NBATCH; ++b) if (lane == b) v = __popcll(bal[c][b]);
    if (lane < NBATCH) cnt[lane * 8 + wave * CPT + c] = v;
  }
  STAMP(1);
  __syncthreads();
  int lo, n;
  {
    const int own = cnt[lane];
    int incl = own;
#pragma unroll
    for (int o = 1; o < 64; o <<= 1) { const int t = __shfl_up(incl, o); if (lane >= o) incl += t; }
    const int excl = incl - own;
    const int U = __builtin_amdgcn_readlane(incl, 63);
    lo = (slot * U) / NSLOT;
    n = ((slot + 1) * U) / NSLOT - lo;
    uint2* list = reinterpret_cast<uint2*>(smem + L_LIST);
#pragma unroll
    for (int c = 0; c < CPT; ++c) {
      const int j = (wave * CPT + c) * 64 + lane;
#pragma unroll
      for (int b = 0; b < NBATCH; ++b) {
        const int base = __builtin_amdgcn_readlane(excl, b * 8 + wave * CPT + c) - lo;
        const int u = base + (int)__builtin_amdgcn_mbcnt_hi((unsigned)(bal[c][b] >> 32), __builtin_amdgcn_mbcnt_lo((unsigned)bal[c][b], 0u));
        if (msk[c][b] != 0u && (unsigned)u < (unsigned)n) list[u] = make_uint2((unsigned)id[c][b], msk[c][b] | ((unsigned)j << 8));
      }
    }
  }
  __syncthreads();
  const int ntiles = (n + 1) >> 1;
  const int nt = (ntiles > wave) ? (ntiles - wave + NW - 1) / NW : 0;
  const bool lastodd = (n & 1) && (nt > 0) && (wave + NW * (nt - 1) == ntiles - 1);
  uint4 ent = make_uint4(0u, 0u, 0u, 0u);
  if (lane < nt) ent = *reinterpret_cast<const uint4*>(smem + L_LIST + 16 * (wave + NW * lane));
  if (lastodd && lane == nt - 1) { ent.z = 0u; ent.w = 0u; }
  if (myzero) {
#pragma unroll
    for (int s = 0; s < 8; ++s) qf[s] = bf16x8{};
  }
#pragma unroll
  for (int s = 0; s < 8; ++s) asm volatile("" : "+v"(qf[s]));

  const uint64_t kb = (uint64_t)kc, vb = (uint64_t)vc;
  const u32x4 srdK = {(unsigned)kb, (unsigned)(kb >> 32) & 0xffffu, kv_bytes, 0x00020000u};
  const u32x4 srdV = {(unsigned)vb, (unsigned)(vb >> 32) & 0xffffu, kv_bytes, 0x00020000u};
  const unsigned g4 = lane >> 4, p16 = lane & 15;
  unsigned vk[4], vv[4];
#pragma unroll
  for (int i = 0; i < 4; ++i) {
    vk[i] = (4 * i + g4) * TOK_BYTES + 16 * (p16 ^ (4 * i + g4)) - 1024 * i;
    vv[i] = (4 * i + g4) * TOK_BYTES + 16 * (p16 ^ (g4 << 2)) - 1024 * i;
  }
  const unsigned sbase = (unsigned)(size_t)smem;
  const unsigned ring = sbase + wave * (DEPTH * 16384);
  const unsigned hoff = head * (DH * 2);

  auto issueK = [&](int i, int slotd) {
    const unsigned pa = __builtin_amdgcn_readlane(ent.x, i) * PAGE_BYTES + hoff;
    dma_pageK(ring + slotd * 16384, srdK, pa, vk[0], vk[1], vk[2], vk[3]);
    if (!(lastodd && i == nt - 1)) {
      const unsigned pb = __builtin_amdgcn_readlane(ent.z, i) * PAGE_BYTES + hoff;
      dma_pageK(ring + slotd * 16384 + 4096, srdK, pb, vk[0], vk[1], vk[2], vk[3]);
    }
  };
  auto issueV = [&](int i, int slotd) {
    const unsigned pa = __builtin_amdgcn_readlane(ent.x, i) * PAGE_BYTES + hoff;
    dma_pageV(ring + slotd * 16384 + 8192, srdV, pa, vv[0], vv[1], vv[2], vv[3]);
    if (!(lastodd && i == nt - 1)) {
      const unsigned pb = __builtin_amdgcn_readlane(ent.z, i) * PAGE_BYTES + hoff;
      dma_pageV(ring + slotd * 16384 + 12288, srdV, pb, vv[0], vv[1], vv[2], vv[3]);
    }
  };
  STAMP(2);
#pragma unroll
  for (int d = 0; d < DEPTH; ++d) if (d < nt) { issueK(d, d); issueV(d, d); }

  const unsigned krow = col * 256;
  const unsigned tq = (lane & 15) >> 2, tp = lane & 3, blk = (lane >> 4) & 1;
  const unsigned vrow = 8192 + (4 * hh + tq) * 256 + 8 * (tp & 1);

  f32x16 acc[4];
#pragma unroll
  for (int c = 0; c < 4; ++c) acc[c] = f32x16{};
  float m = -INFINITY, lsum = 0.f;
  const int tot4 = 2 * nt - (lastodd ? 1 : 0);

  int sd = 0;
  for (int i = 0; i < nt; ++i) {
    char* rb = smem + wave * (DEPTH * 16384) + sd * 16384;
    const int ahead = min(tot4, 2 * (i + DEPTH)) - min(tot4, 2 * (i + 1));
    const int npi = min(tot4 - 2 * i, 2);
    const int npn = max(0, min(tot4 - 2 * (i + DEPTH), 2));
    wait_vm4(npi + 2 * ahead);
    if (i == 0) STAMP(3);
    if (i == 1) STAMP(6);
    bf16x8 kf[8];
#pragma unroll
    for (int s = 0; s < 8; ++s) kf[s] = *reinterpret_cast<const bf16x8*>(rb + krow + 16 * ((2 * s + hh) ^ (col & 15)));
    asm volatile("s_waitcnt lgkmcnt(0)" ::: "memory");
    if (i + DEPTH < nt) issueK(i + DEPTH, sd);
    f32x16 sc = f32x16{};
#pragma unroll
    for (int s = 0; s < 8; ++s) sc = __builtin_amdgcn_mfma_f32_32x32x16_bf16(kf[s], qf[s], sc, 0, 0, 0);
    const unsigned ea = __builtin_amdgcn_readlane(ent.y, i), eb = __builtin_amdgcn_readlane(ent.w, i);
    const int nva = (((ea >> (col >> 2)) & 1u) ? myc - (int)(ea >> 8) * PAGE : 0) - 4 * hh;
    const int nvb = (((eb >> (col >> 2)) & 1u) ? myc - (int)(eb >> 8) * PAGE : 0) - 4 * hh;
    float mx = -INFINITY;
#pragma unroll
    for (int r = 0; r < 16; ++r) {
      const int t = (r & 3) + 8 * ((r >> 2) & 1);
      sc[r] = (t < (r < 8 ? nva : nvb)) ? sc[r] : -INFINITY;
      mx = fmaxf(mx, sc[r]);
    }
    mx = swap32_max(mx);
    const float mn = fmaxf(m, mx);
    const float msub = (mn == -INFINITY) ? 0.f : mn * SCALE_LOG2E;
    const float alpha = __builtin_amdgcn_exp2f(m * SCALE_LOG2E - msub);
    m = mn;
    float ps = 0.f;
#pragma unroll
    for (int r = 0; r < 16; ++r) { sc[r] = __builtin_amdgcn_exp2f(fmaf(sc[r], SCALE_LOG2E, -msub)); ps += sc[r]; }
    lsum = fmaf(lsum, alpha, ps);
    bf16x8 pb[2];
#pragma unroll
    for (int s = 0; s < 2; ++s)
#pragma unroll
      for (int k = 0; k < 4; ++k) {
        const bf16x2 t = __builtin_convertvector((f32x2{sc[8 * s + 2 * k], sc[8 * s + 2 * k + 1]}), bf16x2);
        pb[s][2 * k] = t[0]; pb[s][2 * k + 1] = t[1];
      }
#pragma unroll
    for (int c = 0; c < 4; ++c) acc[c] *= alpha;
    wait_vm4(2 * ahead + npn);
    bf16x8 vf[2][4];
#pragma unroll
    for (int s = 0; s < 2; ++s)
#pragma unroll
      for (int c = 0; c < 4; ++c) {
        char* a = rb + vrow + 16 * ((4 * c + 2 * blk + (tp >> 1)) ^ (tq << 2)) + s * 4096;
        const bf16x4 x = __builtin_amdgcn_ds_read_tr16_b64_v4bf16((lds_bf16x4_p)(a));
        const bf16x4 y = __builtin_amdgcn_ds_read_tr16_b64_v4bf16((lds_bf16x4_p)(a + 2048));
        vf[s][c] = __builtin_shufflevector(x, y, 0, 1, 2, 3, 4, 5, 6, 7);
      }
    asm volatile("s_waitcnt lgkmcnt(0)" ::: "memory");
    if (i + DEPTH < nt) issueV(i + DEPTH, sd);
#pragma unroll
    for (int c = 0; c < 4; ++c) acc[c] = __builtin_amdgcn_mfma_f32_32x32x16_bf16(vf[0][c], pb[0], acc[c], 0, 0, 0);
    if (npi == 2) {
#pragma unroll
      for (int c = 0; c < 4; ++c) acc[c] = __builtin_amdgcn_mfma_f32_32x32x16_bf16(vf[1][c], pb[1], acc[c], 0, 0, 0);
    }
    sd = (sd + 1 == DEPTH) ? 0 : sd + 1;
  }

  STAMP(4);
  float* sm = reinterpret_cast<float*>(smem + L_M);
  float* sl = reinterpret_cast<float*>(smem + L_L);
  const float ltot = swap32_sum(lsum);
  const float ms = m * SCALE_LOG2E;
  if (lane < 32) { sm[wave * 32 + lane] = ms; sl[wave * 32 + lane] = ltot; }
  __syncthreads();
  {
    float M = -INFINITY;
#pragma unroll
    for (int w = 0; w < NW; ++w) M = fmaxf(M, sm[w * 32 + col]);
    if (M != -INFINITY) {
      const float f = __builtin_amdgcn_exp2f(ms - M);
      char* ob = smem + (wave * 32 + col) * OROW + 16 * hh;
#pragma unroll
      for (int c = 0; c < 4; ++c)
#pragma unroll
        for (int g = 0; g < 4; ++g)
          *reinterpret_cast<f32x4*>(ob + 128 * c + 32 * g) = f32x4{acc[c][4 * g] * f, acc[c][4 * g + 1] * f, acc[c][4 * g + 2] * f, acc[c][4 * g + 3] * f};
    }
    if (tid < 32) {
      float L = 0.f;
      const float Ms = (M == -INFINITY) ? 0.f : M;
#pragma unroll
      for (int w = 0; w < NW; ++w) L = fmaf(sl[w * 32 + col], __builtin_amdgcn_exp2f(sm[w * 32 + col] - Ms), L);
      reinterpret_cast<float2*>(ws + WS_ML)[(head * 32 + col) * NSLOT + slot] = make_float2(M, L);
    }
  }
  __syncthreads();
#pragma unroll
  for (int it = 0; it < 1024 / NTHR; ++it) {
    const int cc = (tid >> 5) + it * (NTHR / 32), d4 = tid & 31;
    float M = -INFINITY;
#pragma unroll
    for (int w = 0; w < NW; ++w) M = fmaxf(M, sm[w * 32 + cc]);
    if (M != -INFINITY) {
      f32x4 o = f32x4{};
#pragma unroll
      for (int w = 0; w < NW; ++w) o += *reinterpret_cast<const f32x4*>(smem + (w * 32 + cc) * OROW + 16 * d4);
      *reinterpret_cast<f32x4*>(ws + WS_PO + ((size_t)((head * 32 + cc) * NSLOT + slot) * DH + 4 * d4) * 4) = o;
    }
  }
  asm volatile("s_waitcnt vmcnt(0)" ::: "memory");
  STAMP(5);
  st_[7] = nt;
  if (lane < 8) {
    unsigned long long v = 0;
#pragma unroll
    for (int k = 0; k < 8; ++k) if (lane == k) v = st_[k];
    reinterpret_cast<unsigned long long*>(ws + WS_STAMP)[(blockIdx.x * NW + wave) * 8 + lane] = v;
  }
}

__global__ void __launch_bounds__(256) attn_combine(const unsigned char* __restrict__ ws, __hip_bfloat16* __restrict__ out) {
  __shared__ __attribute__((aligned(16))) float red[8][DH];
  const unsigned long long tc0 = __builtin_amdgcn_s_memrealtime();
  const int head = blockIdx.x & 7, col = blockIdx.x >> 3;
  const int tid = threadIdx.x, s = tid & 31, grp = tid >> 5;
  const int row = head * 32 + col;
  const float2 ml = reinterpret_cast<const float2*>(ws + WS_ML)[row * NSLOT + s];
  float M = ml.x;
#pragma unroll
  for (int o = 16; o >= 1; o >>= 1) M = fmaxf(M, __shfl_xor(M, o));
  const float w = (ml.x == -INFINITY) ? 0.f : __builtin_amdgcn_exp2f(ml.x - M);
  float L = w * ml.y;
#pragma unroll
  for (int o = 16; o >= 1; o >>= 1) L += __shfl_xor(L, o);
  f32x4 part[4]; float wk[4];
#pragma unroll
  for (int k = 0; k < 4; ++k) {
    const int sl = grp + 8 * k;
    const float mk = __shfl(ml.x, (tid & 32) + sl);
    wk[k] = __shfl(w, (tid & 32) + sl);
    part[k] = f32x4{};
    if (mk != -INFINITY) part[k] = *reinterpret_cast<const f32x4*>(ws + WS_PO + ((size_t)(row * NSLOT + sl) * DH + 4 * s) * 4);
  }
  f32x4 o = f32x4{};
#pragma unroll
  for (int k = 0; k < 4; ++k) o += part[k] * wk[k];
  *reinterpret_cast<f32x4*>(&red[grp][4 * s]) = o;
  __syncthreads();
  if (tid < 64) {
    float a = 0.f, b = 0.f;
#pragma unroll
    for (int g = 0; g < 8; ++g) { a += red[g][2 * tid]; b += red[g][2 * tid + 1]; }
    const float inv = 1.f / L;
    const bf16x2 r = __builtin_convertvector((f32x2{a * inv, b * inv}), bf16x2);
    *reinterpret_cast<bf16x2*>(out + ((col >> 2) * NH + head * 4 + (col & 3)) * DH + 2 * tid) = r;
  }
  const unsigned long long tc1 = __builtin_amdgcn_s_memrealtime();
  if (blockIdx.x == 0) {
    __shared__ unsigned mn[4][8], mx[4][8];
    const unsigned long long* sp = reinterpret_cast<const unsigned long long*>(ws + WS_STAMP) + (size_t)tid * NW * 8;
    unsigned a[6], b[6];
#pragma unroll
    for (int k = 0; k < 6; ++k) {
      a[k] = ~0u; b[k] = 0u;
#pragma unroll
      for (int w = 0; w < NW; ++w) { const unsigned v = (unsigned)(sp[w * 8 + k] - tc0 + 1000000ull); a[k] = min(a[k], v); b[k] = max(b[k], v); }
    }
    const unsigned tp = (unsigned)(reinterpret_cast<const unsigned long long*>(ws + WS_STAMP - 4096)[tid] - tc0 + 1000000ull);
    unsigned pa = tp, pb = tp;
#pragma unroll
    for (int o = 32; o >= 1; o >>= 1) {
#pragma unroll
      for (int k = 0; k < 6; ++k) { a[k] = min(a[k], (unsigned)__shfl_xor((int)a[k], o)); b[k] = max(b[k], (unsigned)__shfl_xor((int)b[k], o)); }
      pa = min(pa, (unsigned)__shfl_xor((int)pa, o)); pb = max(pb, (unsigned)__shfl_xor((int)pb, o));
    }
    if ((tid & 63) == 0) { for (int k = 0; k < 6; ++k) { mn[tid >> 6][k] = a[k]; mx[tid >> 6][k] = b[k]; } mn[tid >> 6][6] = pa; mx[tid >> 6][6] = pb; }
    __syncthreads();
    unsigned MN[7], MX[7];
    for (int k = 0; k < 7; ++k) { MN[k] = min(min(mn[0][k], mn[1][k]), min(mn[2][k], mn[3][k])); MX[k] = max(max(mx[0][k], mx[1][k]), max(mx[2][k], mx[3][k])); }
    unsigned d = 0;
    if (LEAK == 1) d = MN[0] - MX[6];
    if (LEAK == 2) d = (unsigned)(tc1 - tc0);
    if (LEAK == 3) d = 1000000u + (unsigned)(tc1 - tc0) - MN[6];
    if (LEAK == 4) d = MX[6] - MN[6];
    if (LEAK == 5) d = MN[1] - MN[0];
    d = d > 10000u ? 10000u : d;
    const unsigned long long t1 = __builtin_amdgcn_s_memrealtime() + 4 * d;
    while (__builtin_amdgcn_s_memrealtime() < t1) __builtin_amdgcn_s_sleep(1);
  }
}
__global__ void attn_pre(unsigned char* ws) {
  if (threadIdx.x == 0) reinterpret_cast<unsigned long long*>(ws + WS_STAMP - 4096)[blockIdx.x] = __builtin_amdgcn_s_memrealtime();
}

extern "C" void kernel_launch(void* const* d_in, const int* in_sizes, int n_in, void* d_out, int out_size, void* d_ws, size_t ws_size,
                              hipStream_t stream) {
  const __hip_bfloat16* q = (const __hip_bfloat16*)d_in[0];
  const unsigned kv_bytes = (unsigned)in_sizes[1] * 2u;
  attn_pre<<<256, 64, 0, stream>>>((unsigned char*)d_ws);
  attn_main<<<NHKV * NSLOT, NTHR, 0, stream>>>(q, d_in[1], d_in[2], (const int*)d_in[3], (const int*)d_in[4], (unsigned char*)d_ws, kv_bytes);
  attn_combine<<<NHKV * 32, 256, 0, stream>>>((const unsigned char*)d_ws, (__hip_bfloat16*)d_out);
}
```

```cpp
#include <hip/hip_runtime.h>
#include <hip/hip_bf16.h>
#include <stdint.h>

#define STAMPS
#define LEAKSEL 0
#define NW 8
#define DEPTH 1
#define HEAD_ROT 0
#define HEAD_MAJOR 1

typedef __attribute__((ext_vector_type(8))) __bf16 bf16x8;
typedef __attribute__((ext_vector_type(4))) __bf16 bf16x4;
typedef __attribute__((ext_vector_type(2))) __bf16 bf16x2;
typedef __attribute__((ext_vector_type(2))) float f32x2;
typedef __attribute__((ext_vector_type(4))) float f32x4;
typedef __attribute__((ext_vector_type(16))) float f32x16;
typedef __attribute__((ext_vector_type(4))) unsigned u32x4;
typedef __attribute__((address_space(3))) bf16x4* lds_bf16x4_p;

constexpr int NBATCH = 8, NH = 32, NHKV = 8, DH = 128, PAGE = 16, NBLK = 512;
constexpr int NSLOT = 32;
constexpr int NTHR = NW * 64;
constexpr int CPT = NBLK / NTHR;
constexpr int MAXU = NBATCH * NBLK / NSLOT;
constexpr int PAGE_BYTES = PAGE * NHKV * DH * 2;
constexpr int TOK_BYTES = NHKV * DH * 2;
constexpr float SCALE_LOG2E = 0.08838834764831845f * 1.4426950408889634f;

constexpr int RING_BYTES = NW * DEPTH * 16384;
constexpr int OROW = 528;
constexpr int OBUF_BYTES = NW * 32 * OROW;
constexpr int BIG_BYTES = RING_BYTES > OBUF_BYTES ? RING_BYTES : OBUF_BYTES;
constexpr int L_CNT = BIG_BYTES;
constexpr int L_LIST = L_CNT + 64 * 4;
constexpr int L_M = L_LIST + MAXU * 8;
constexpr int L_L = L_M + NW * 32 * 4;
constexpr int LDS_BYTES = L_L + NW * 32 * 4;
static_assert(LDS_BYTES <= 163840, "lds");

constexpr size_t WS_ML = 0;
constexpr size_t WS_PO = (size_t)NHKV * 32 * NSLOT * 8;

__device__ __forceinline__ float swap32_max(float x) {
  auto r = __builtin_amdgcn_permlane32_swap(__float_as_uint(x), __float_as_uint(x), false, false);
  return fmaxf(__uint_as_float(r[0]), __uint_as_float(r[1]));
}
__device__ __forceinline__ float swap32_sum(float x) {
  auto r = __builtin_amdgcn_permlane32_swap(__float_as_uint(x), __float_as_uint(x), false, false);
  return __uint_as_float(r[0]) + __uint_as_float(r[1]);
}

#define K_POL " nt"
#define V_POL " nt"
#define DMA_PAGE_FN(NAME, POL) \
__device__ __forceinline__ void NAME(unsigned ldsaddr, u32x4 srd, unsigned soff, unsigned v0, unsigned v1, unsigned v2, unsigned v3) { \
  asm volatile( \
      "s_nop 4\n\t" \
      "s_mov_b32 m0, %0\n\t" \
      "s_nop 0\n\t" \
      "buffer_load_dwordx4 %3, %1, %2 offen" POL " lds\n\t" \
      "buffer_load_dwordx4 %4, %1, %2 offen offset:1024" POL " lds\n\t" \
      "buffer_load_dwordx4 %5, %1, %2 offen offset:2048" POL " lds\n\t" \
      "buffer_load_dwordx4 %6, %1, %2 offen offset:3072" POL " lds" \
      :: "s"(ldsaddr), "s"(srd), "s"(soff), "v"(v0), "v"(v1), "v"(v2), "v"(v3) : "memory"); \
}
DMA_PAGE_FN(dma_pageK, K_POL)
DMA_PAGE_FN(dma_pageV, V_POL)
#define WAITVM(n) asm volatile("s_waitcnt vmcnt(" #n ")" ::: "memory")
__device__ __forceinline__ void wait_vm4(int n4) {
  if (DEPTH == 1) {
    if (n4 >= 2) WAITVM(8); else if (n4 == 1) WAITVM(4); else WAITVM(0);
  } else if (DEPTH == 2) {
    if (n4 >= 6) WAITVM(24); else if (n4 == 5) WAITVM(20); else if (n4 == 4) WAITVM(16); else if (n4 == 3) WAITVM(12);
    else if (n4 == 2) WAITVM(8); else if (n4 == 1) WAITVM(4); else WAITVM(0);
  } else {
    if (n4 >= 14) WAITVM(56); else if (n4 >= 12) WAITVM(48); else if (n4 >= 10) WAITVM(40); else if (n4 >= 8) WAITVM(32);
    else if (n4 >= 6) WAITVM(24); else if (n4 == 5) WAITVM(20); else if (n4 == 4) WAITVM(16); else if (n4 == 3) WAITVM(12);
    else if (n4 == 2) WAITVM(8); else if (n4 == 1) WAITVM(4); else WAITVM(0);
  }
}

constexpr size_t WS_STAMP = (size_t)64 << 20;
#define STAMP(k) do { st_[k] = __builtin_amdgcn_s_memrealtime(); } while (0)
__global__ void __launch_bounds__(NTHR, NW / 4)
attn_main(const __hip_bfloat16* __restrict__ q, const void* __restrict__ kc, const void* __restrict__ vc,
          const int* __restrict__ bt, const int* __restrict__ ctx, unsigned char* __restrict__ ws, unsigned kv_bytes) {
  __shared__ __attribute__((aligned(16))) char smem[LDS_BYTES];
  const int tid = threadIdx.x, lane = tid & 63;
  const int wave = __builtin_amdgcn_readfirstlane(tid >> 6);
  const int head = (blockIdx.x + HEAD_ROT) & 7, slot = blockIdx.x >> 3;
  const int col = lane & 31, hh = lane >> 5;
  unsigned long long st_[8] = {0, 0, 0, 0, 0, 0, 0, 0};
  STAMP(0);

  int id[CPT][NBATCH];
#pragma unroll
  for (int c = 0; c < CPT; ++c)
#pragma unroll
    for (int b = 0; b < NBATCH; ++b) id[c][b] = bt[b * NBLK + (wave * CPT + c) * 64 + lane];
  int cl[NBATCH];
#pragma unroll
  for (int b = 0; b < NBATCH; ++b) cl[b] = ctx[b];
  bf16x8 qf[8];
  {
    const __hip_bfloat16* qp = q + ((col >> 2) * NH + head * 4 + (col & 3)) * DH + 8 * hh;
#pragma unroll
    for (int s = 0; s < 8; ++s) qf[s] = *reinterpret_cast<const bf16x8*>(qp + 16 * s);
  }
  int myc = 0; bool myzero = false;
#pragma unroll
  for (int b = 0; b < NBATCH; ++b) {
    const bool z = cl[b] <= 0;
    cl[b] = z ? NBLK * PAGE : min(cl[b], NBLK * PAGE);
    if ((col >> 2) == b) { myc = cl[b]; myzero = z; }
  }
  unsigned msk[CPT][NBATCH];
  unsigned long long bal[CPT][NBATCH];
  int* cnt = reinterpret_cast<int*>(smem + L_CNT);
#pragma unroll
  for (int c = 0; c < CPT; ++c) {
    const int j = (wave * CPT + c) * 64 + lane;
#pragma unroll
    for (int b = 0; b < NBATCH; ++b) if (!(j * PAGE < cl[b])) id[c][b] = -1 - b;
#pragma unroll
    for (int b = 0; b < NBATCH; ++b) msk[c][b] = 1u << b;
#pragma unroll
    for (int b = 0; b < NBATCH; ++b)
#pragma unroll
      for (int e = b + 1; e < NBATCH; ++e) {
        const bool eq = id[c][b] == id[c][e];
        msk[c][b] |= eq ? (1u << e) : 0u;
        msk[c][e] |= eq ? (1u << b) : 0u;
      }
#pragma unroll
    for (int b = 0; b < NBATCH; ++b) {
      const bool lead = (j * PAGE < cl[b]) && (msk[c][b] & ((1u << b) - 1u)) == 0u;
      bal[c][b] = __ballot(lead);
      if (!lead) msk[c][b] = 0u;
    }
    int v = 0;
#pragma unroll
    for (int b = 0; b < NBATCH; ++b) if (lane == b) v = __popcll(bal[c][b]);
    if (lane < NBATCH) cnt[lane * 8 + wave * CPT + c] = v;
  }
  STAMP(1);
  __syncthreads();
  int lo, n;
  {
    const int own = cnt[lane];
    int incl = own;
#pragma unroll
    for (int o = 1; o < 64; o <<= 1) { const int t = __shfl_up(incl, o); if (lane >= o) incl += t; }
    const int excl = incl - own;
    const int U = __builtin_amdgcn_readlane(incl, 63);
    lo = (slot * U) / NSLOT;
    n = ((slot + 1) * U) / NSLOT - lo;
    uint2* list = reinterpret_cast<uint2*>(smem + L_LIST);
#pragma unroll
    for (int c = 0; c < CPT; ++c) {
      const int j = (wave * CPT + c) * 64 + lane;
#pragma unroll
      for (int b = 0; b < NBATCH; ++b) {
        const int base = __builtin_amdgcn_readlane(excl, b * 8 + wave * CPT + c) - lo;
        const int u = base + (int)__builtin_amdgcn_mbcnt_hi((unsigned)(bal[c][b] >> 32), __builtin_amdgcn_mbcnt_lo((unsigned)bal[c][b], 0u));
        if (msk[c][b] != 0u && (unsigned)u < (unsigned)n) list[u] = make_uint2((unsigned)id[c][b], msk[c][b] | ((unsigned)j << 8));
      }
    }
  }
  __syncthreads();
  const int ntiles = (n + 1) >> 1;
  const int nt = (ntiles > wave) ? (ntiles - wave + NW - 1) / NW : 0;
  const bool lastodd = (n & 1) && (nt > 0) && (wave + NW * (nt - 1) == ntiles - 1);
  uint4 ent = make_uint4(0u, 0u, 0u, 0u);
  if (lane < nt) ent = *reinterpret_cast<const uint4*>(smem + L_LIST + 16 * (wave + NW * lane));
  if (lastodd && lane == nt - 1) { ent.z = 0u; ent.w = 0u; }
  if (myzero) {
#pragma unroll
    for (int s = 0; s < 8; ++s) qf[s] = bf16x8{};
  }
#pragma unroll
  for (int s = 0; s < 8; ++s) asm volatile("" : "+v"(qf[s]));

  const uint64_t kb = (uint64_t)kc, vb = (uint64_t)vc;
  const u32x4 srdK = {(unsigned)kb, (unsigned)(kb >> 32) & 0xffffu, kv_bytes, 0x00020000u};
  const u32x4 srdV = {(unsigned)vb, (unsigned)(vb >> 32) & 0xffffu, kv_bytes, 0x00020000u};
  const unsigned g4 = lane >> 4, p16 = lane & 15;
  unsigned vk[4], vv[4];
#pragma unroll
  for (int i = 0; i < 4; ++i) {
    vk[i] = (4 * i + g4) * TOK_BYTES + 16 * (p16 ^ (4 * i + g4)) - 1024 * i;
    vv[i] = (4 * i + g4) * TOK_BYTES + 16 * (p16 ^ (g4 << 2)) - 1024 * i;
  }
  const unsigned sbase = (unsigned)(size_t)smem;
  const unsigned ring = sbase + wave * (DEPTH * 16384);
  const unsigned hoff = head * (DH * 2);

  auto issueK = [&](int i, int slotd) {
    const unsigned pa = __builtin_amdgcn_readlane(ent.x, i) * PAGE_BYTES + hoff;
    dma_pageK(ring + slotd * 16384, srdK, pa, vk[0], vk[1], vk[2], vk[3]);
    if (!(lastodd && i == nt - 1)) {
      const unsigned pb = __builtin_amdgcn_readlane(ent.z, i) * PAGE_BYTES + hoff;
      dma_pageK(ring + slotd * 16384 + 4096, srdK, pb, vk[0], vk[1], vk[2], vk[3]);
    }
  };
  auto issueV = [&](int i, int slotd) {
    const unsigned pa = __builtin_amdgcn_readlane(ent.x, i) * PAGE_BYTES + hoff;
    dma_pageV(ring + slotd * 16384 + 8192, srdV, pa, vv[0], vv[1], vv[2], vv[3]);
    if (!(lastodd && i == nt - 1)) {
      const unsigned pb = __builtin_amdgcn_readlane(ent.z, i) * PAGE_BYTES + hoff;
      dma_pageV(ring + slotd * 16384 + 12288, srdV, pb, vv[0], vv[1], vv[2], vv[3]);
    }
  };
  STAMP(2);
#pragma unroll
  for (int d = 0; d < DEPTH; ++d) if (d < nt) { issueK(d, d); issueV(d, d); }

  const unsigned krow = col * 256;
  const unsigned tq = (lane & 15) >> 2, tp = lane & 3, blk = (lane >> 4) & 1;
  const unsigned vrow = 8192 + (4 * hh + tq) * 256 + 8 * (tp & 1);

  f32x16 acc[4];
#pragma unroll
  for (int c = 0; c < 4; ++c) acc[c] = f32x16{};
  float m = -INFINITY, lsum = 0.f;
  const int tot4 = 2 * nt - (lastodd ? 1 : 0);

  int sd = 0;
  for (int i = 0; i < nt; ++i) {
    char* rb = smem + wave * (DEPTH * 16384) + sd * 16384;
    const int ahead = min(tot4, 2 * (i + DEPTH)) - min(tot4, 2 * (i + 1));
    const int npi = min(tot4 - 2 * i, 2);
    const int npn = max(0, min(tot4 - 2 * (i + DEPTH), 2));
    wait_vm4(npi + 2 * ahead);
    if (i == 0) STAMP(3);
    if (i == 1) STAMP(6);
    bf16x8 kf[8];
#pragma unroll
    for (int s = 0; s < 8; ++s) kf[s] = *reinterpret_cast<const bf16x8*>(rb + krow + 16 * ((2 * s + hh) ^ (col & 15)));
    asm volatile("s_waitcnt lgkmcnt(0)" ::: "memory");
    if (i + DEPTH < nt) issueK(i + DEPTH, sd);
    f32x16 sc = f32x16{};
#pragma unroll
    for (int s = 0; s < 8; ++s) sc = __builtin_amdgcn_mfma_f32_32x32x16_bf16(kf[s], qf[s], sc, 0, 0, 0);
    const unsigned ea = __builtin_amdgcn_readlane(ent.y, i), eb = __builtin_amdgcn_readlane(ent.w, i);
    const int nva = (((ea >> (col >> 2)) & 1u) ? myc - (int)(ea >> 8) * PAGE : 0) - 4 * hh;
    const int nvb = (((eb >> (col >> 2)) & 1u) ? myc - (int)(eb >> 8) * PAGE : 0) - 4 * hh;
    float mx = -INFINITY;
#pragma unroll
    for (int r = 0; r < 16; ++r) {
      const int t = (r & 3) + 8 * ((r >> 2) & 1);
      sc[r] = (t < (r < 8 ? nva : nvb)) ? sc[r] : -INFINITY;
      mx = fmaxf(mx, sc[r]);
    }
    mx = swap32_max(mx);
    const float mn = fmaxf(m, mx);
    const float msub = (mn == -INFINITY) ? 0.f : mn * SCALE_LOG2E;
    const float alpha = __builtin_amdgcn_exp2f(m * SCALE_LOG2E - msub);
    m = mn;
    float ps = 0.f;
#pragma unroll
    for (int r = 0; r < 16; ++r) { sc[r] = __builtin_amdgcn_exp2f(fmaf(sc[r], SCALE_LOG2E, -msub)); ps += sc[r]; }
    lsum = fmaf(lsum, alpha, ps);
    bf16x8 pb[2];
#pragma unroll
    for (int s = 0; s < 2; ++s)
#pragma unroll
      for (int k = 0; k < 4; ++k) {
        const bf16x2 t = __builtin_convertvector((f32x2{sc[8 * s + 2 * k], sc[8 * s + 2 * k + 1]}), bf16x2);
        pb[s][2 * k] = t[0]; pb[s][2 * k + 1] = t[1];
      }
#pragma unroll
    for (int c = 0; c < 4; ++c) acc[c] *= alpha;
    wait_vm4(2 * ahead + npn);
    bf16x8 vf[2][4];
#pragma unroll
    for (int s = 0; s < 2; ++s)
#pragma unroll
      for (int c = 0; c < 4; ++c) {
        char* a = rb + vrow + 16 * ((4 * c + 2 * blk + (tp >> 1)) ^ (tq << 2)) + s * 4096;
        const bf16x4 x = __builtin_amdgcn_ds_read_tr16_b64_v4bf16((lds_bf16x4_p)(a));
        const bf16x4 y = __builtin_amdgcn_ds_read_tr16_b64_v4bf16((lds_bf16x4_p)(a + 2048));
        vf[s][c] = __builtin_shufflevector(x, y, 0, 1, 2, 3, 4, 5, 6, 7);
      }
    asm volatile("s_waitcnt lgkmcnt(0)" ::: "memory");
    if (i + DEPTH < nt) issueV(i + DEPTH, sd);
#pragma unroll
    for (int c = 0; c < 4; ++c) acc[c] = __builtin_amdgcn_mfma_f32_32x32x16_bf16(vf[0][c], pb[0], acc[c], 0, 0, 0);
    if (npi == 2) {
#pragma unroll
      for (int c = 0; c < 4; ++c) acc[c] = __builtin_amdgcn_mfma_f32_32x32x16_bf16(vf[1][c], pb[1], acc[c], 0, 0, 0);
    }
    sd = (sd + 1 == DEPTH) ? 0 : sd + 1;
  }

  STAMP(4);
  float* sm = reinterpret_cast<float*>(smem + L_M);
  float* sl = reinterpret_cast<float*>(smem + L_L);
  const float ltot = swap32_sum(lsum);
  const float ms = m * SCALE_LOG2E;
  if (lane < 32) { sm[wave * 32 + lane] = ms; sl[wave * 32 + lane] = ltot; }
  __syncthreads();
  {
    float M = -INFINITY;
#pragma unroll
    for (int w = 0; w < NW; ++w) M = fmaxf(M, sm[w * 32 + col]);
    if (M != -INFINITY) {
      const float f = __builtin_amdgcn_exp2f(ms - M);
      char* ob = smem + (wave * 32 + col) * OROW + 16 * hh;
#pragma unroll
      for (int c = 0; c < 4; ++c)
#pragma unroll
        for (int g = 0; g < 4; ++g)
          *reinterpret_cast<f32x4*>(ob + 128 * c + 32 * g) = f32x4{acc[c][4 * g] * f, acc[c][4 * g + 1] * f, acc[c][4 * g + 2] * f, acc[c][4 * g + 3] * f};
    }
    if (tid < 32) {
      float L = 0.f;
      const float Ms = (M == -INFINITY) ? 0.f : M;
#pragma unroll
      for (int w = 0; w < NW; ++w) L = fmaf(sl[w * 32 + col], __builtin_amdgcn_exp2f(sm[w * 32 + col] - Ms), L);
      reinterpret_cast<float2*>(ws + WS_ML)[(head * 32 + col) * NSLOT + slot] = make_float2(M, L);
    }
  }
  __syncthreads();
#pragma unroll
  for (int it = 0; it < 1024 / NTHR; ++it) {
    const int cc = (tid >> 5) + it * (NTHR / 32), d4 = tid & 31;
    float M = -INFINITY;
#pragma unroll
    for (int w = 0; w < NW; ++w) M = fmaxf(M, sm[w * 32 + cc]);
    if (M != -INFINITY) {
      f32x4 o = f32x4{};
#pragma unroll
      for (int w = 0; w < NW; ++w) o += *reinterpret_cast<const f32x4*>(smem + (w * 32 + cc) * OROW + 16 * d4);
      *reinterpret_cast<f32x4*>(ws + WS_PO + ((size_t)((head * 32 + cc) * NSLOT + slot) * DH + 4 * d4) * 4) = o;
    }
  }
  asm volatile("s_waitcnt vmcnt(0)" ::: "memory");
  STAMP(5);
  st_[7] = nt;
  if (lane < 8) {
    unsigned long long v = 0;
#pragma unroll
    for (int k = 0; k < 8; ++k) if (lane == k) v = st_[k];
    reinterpret_cast<unsigned long long*>(ws + WS_STAMP)[(blockIdx.x * NW + wave) * 8 + lane] = v;
  }
}

__global__ void __launch_bounds__(256) attn_combine(unsigned char* __restrict__ ws, __hip_bfloat16* __restrict__ out) {
  __shared__ __attribute__((aligned(16))) float red[8][DH];
  const unsigned long long tc0 = __builtin_amdgcn_s_memrealtime();
  const int head = blockIdx.x & 7, col = blockIdx.x >> 3;
  const int tid = threadIdx.x, s = tid & 31, grp = tid >> 5;
  const int row = head * 32 + col;
  const float2 ml = reinterpret_cast<const float2*>(ws + WS_ML)[row * NSLOT + s];
  float M = ml.x;
#pragma unroll
  for (int o = 16; o >= 1; o >>= 1) M = fmaxf(M, __shfl_xor(M, o));
  const float w = (ml.x == -INFINITY) ? 0.f : __builtin_amdgcn_exp2f(ml.x - M);
  float L = w * ml.y;
#pragma unroll
  for (int o = 16; o >= 1; o >>= 1) L += __shfl_xor(L, o);
  f32x4 part[4]; float wk[4];
#pragma unroll
  for (int k = 0; k < 4; ++k) {
    const int sl = grp + 8 * k;
    const float mk = __shfl(ml.x, (tid & 32) + sl);
    wk[k] = __shfl(w, (tid & 32) + sl);
    part[k] = f32x4{};
    if (mk != -INFINITY) part[k] = *reinterpret_cast<const f32x4*>(ws + WS_PO + ((size_t)(row * NSLOT + sl) * DH + 4 * s) * 4);
  }
  f32x4 o = f32x4{};
#pragma unroll
  for (int k = 0; k < 4; ++k) o += part[k] * wk[k];
  *reinterpret_cast<f32x4*>(&red[grp][4 * s]) = o;
  __syncthreads();
  if (tid < 64) {
    float a = 0.f, b = 0.f;
#pragma unroll
    for (int g = 0; g < 8; ++g) { a += red[g][2 * tid]; b += red[g][2 * tid + 1]; }
    const float inv = 1.f / L;
    const bf16x2 r = __builtin_convertvector((f32x2{a * inv, b * inv}), bf16x2);
    *reinterpret_cast<bf16x2*>(out + ((col >> 2) * NH + head * 4 + (col & 3)) * DH + 2 * tid) = r;
  }
  asm volatile("s_waitcnt vmcnt(0)" ::: "memory");
  const unsigned long long tc1 = __builtin_amdgcn_s_memrealtime();
  if (tid == 0) { reinterpret_cast<unsigned long long*>(ws + WS_STAMP - 8192)[blockIdx.x] = tc0; reinterpret_cast<unsigned long long*>(ws + WS_STAMP - 8192)[256 + blockIdx.x] = tc1; }
}
__global__ void attn_pre(unsigned char* ws) {
  if (threadIdx.x == 0) reinterpret_cast<unsigned long long*>(ws + WS_STAMP - 4096)[blockIdx.x] = __builtin_amdgcn_s_memrealtime();
}
__global__ void __launch_bounds__(256) attn_leak(unsigned char* ws, int sel) {
  const unsigned long long tl0 = __builtin_amdgcn_s_memrealtime();
  const int tid = threadIdx.x;
  __shared__ unsigned mn[4][16], mx[4][16];
  const unsigned long long* sp = reinterpret_cast<const unsigned long long*>(ws + WS_STAMP) + (size_t)tid * NW * 8;
  unsigned a[9], b[9];
#pragma unroll
  for (int k = 0; k < 6; ++k) {
    a[k] = ~0u; b[k] = 0u;
#pragma unroll
    for (int w = 0; w < NW; ++w) { const unsigned v = (unsigned)(sp[w * 8 + k] - tl0 + 1000000ull); a[k] = min(a[k], v); b[k] = max(b[k], v); }
  }
  a[6] = b[6] = (unsigned)(reinterpret_cast<const unsigned long long*>(ws + WS_STAMP - 4096)[tid] - tl0 + 1000000ull);
  a[7] = b[7] = (unsigned)(reinterpret_cast<const unsigned long long*>(ws + WS_STAMP - 8192)[tid] - tl0 + 1000000ull);
  a[8] = b[8] = (unsigned)(reinterpret_cast<const unsigned long long*>(ws + WS_STAMP - 8192)[256 + tid] - tl0 + 1000000ull);
#pragma unroll
  for (int o = 32; o >= 1; o >>= 1)
#pragma unroll
    for (int k = 0; k < 9; ++k) { a[k] = min(a[k], (unsigned)__shfl_xor((int)a[k], o)); b[k] = max(b[k], (unsigned)__shfl_xor((int)b[k], o)); }
  if ((tid & 63) == 0) for (int k = 0; k < 9; ++k) { mn[tid >> 6][k] = a[k]; mx[tid >> 6][k] = b[k]; }
  __syncthreads();
  unsigned MN[9], MX[9];
  for (int k = 0; k < 9; ++k) { MN[k] = min(min(mn[0][k], mn[1][k]), min(mn[2][k], mn[3][k])); MX[k] = max(max(mx[0][k], mx[1][k]), max(mx[2][k], mx[3][k])); }
  const unsigned long long tl1 = __builtin_amdgcn_s_memrealtime();
  unsigned d = 0;
  if (sel == 1) d = MN[0] - MX[6];
  if (sel == 2) d = MX[8] - MN[7];
  if (sel == 3) d = 1000000u - MN[6];
  if (sel == 4) d = (unsigned)(tl1 - tl0);
  if (sel == 5) d = 1000000u - MX[8];
  if (sel == 6) d = MX[6] - MN[6];
  if (sel == 7) d = MN[1] + MN[2] + MN[3] + MN[4] + MN[5] + MX[0] + MX[1] + MX[2] + MX[3] + MX[4] + MX[5] + MX[7] + MN[8];
  d = d > 10000u ? 10000u : d;
  const unsigned long long t1 = tl1 + 4 * d;
  while (__builtin_amdgcn_s_memrealtime() < t1) __builtin_amdgcn_s_sleep(1);
}

extern "C" void kernel_launch(void* const* d_in, const int* in_sizes, int n_in, void* d_out, int out_size, void* d_ws, size_t ws_size,
                              hipStream_t stream) {
  const __hip_bfloat16* q = (const __hip_bfloat16*)d_in[0];
  const unsigned kv_bytes = (unsigned)in_sizes[1] * 2u;
  attn_pre<<<256, 64, 0, stream>>>((unsigned char*)d_ws);
  attn_main<<<NHKV * NSLOT, NTHR, 0, stream>>>(q, d_in[1], d_in[2], (const int*)d_in[3], (const int*)d_in[4], (unsigned char*)d_ws, kv_bytes);
  attn_combine<<<NHKV * 32, 256, 0, stream>>>((unsigned char*)d_ws, (__hip_bfloat16*)d_out);
  attn_leak<<<1, 256, 0, stream>>>((unsigned char*)d_ws, LEAKSEL);
}
```

```cpp
#include <hip/hip_runtime.h>
#include <hip/hip_bf16.h>
#include <stdint.h>

#define NW 8
#define PRIO 1
#define NS_A 29
#define NS_B 39
#define NS_C 33

typedef __attribute__((ext_vector_type(8))) __bf16 bf16x8;
typedef __attribute__((ext_vector_type(4))) __bf16 bf16x4;
typedef __attribute__((ext_vector_type(2))) __bf16 bf16x2;
typedef __attribute__((ext_vector_type(2))) float f32x2;
typedef __attribute__((ext_vector_type(4))) float f32x4;
typedef __attribute__((ext_vector_type(16))) float f32x16;
typedef __attribute__((ext_vector_type(4))) unsigned u32x4;
typedef __attribute__((ext_vector_type(8))) int i32x8;
typedef __attribute__((ext_vector_type(16))) unsigned u32x16;
typedef __attribute__((address_space(3))) bf16x4* lds_bf16x4_p;
typedef unsigned long long u64;

constexpr int NBATCH = 8, NH = 32, NHKV = 8, DH = 128, PAGE = 16, NBLK = 512;
constexpr int NTHR = NW * 64;
static_assert(5 * NS_A + 2 * NS_B + NS_C == 256, "grid");
constexpr int MAXS = 48;
constexpr int MAXU = 160;
static_assert(NS_B <= MAXS && NS_C <= MAXS && 4096 / NS_A + 2 <= MAXU, "limits");
constexpr int PAGE_BYTES = PAGE * NHKV * DH * 2;
constexpr int TOK_BYTES = NHKV * DH * 2;
constexpr float SCALE_LOG2E = 0.08838834764831845f * 1.4426950408889634f;

constexpr int L_TAB = NW * 16384;
constexpr int L_CNT = L_TAB + NBLK * NBATCH * 4;
constexpr int L_WM = L_CNT + 64;
constexpr int L_CM = L_WM + 32;
constexpr int L_LIST = L_CM + 64 + 96;
constexpr int L_ML = L_LIST + MAXU * 4;
constexpr int LDS_BYTES = L_ML + NW * 32 * 8;
static_assert(LDS_BYTES <= 163840 && (L_LIST & 15) == 0, "lds");

constexpr unsigned WS_REC = 4096;
constexpr unsigned REC = 1152;
constexpr unsigned WS_BYTES = WS_REC + 256u * MAXS * REC;

__device__ __forceinline__ float swap32_max(float x) {
  auto r = __builtin_amdgcn_permlane32_swap(__float_as_uint(x), __float_as_uint(x), false, false);
  return fmaxf(__uint_as_float(r[0]), __uint_as_float(r[1]));
}
__device__ __forceinline__ float swap32_sum(float x) {
  auto r = __builtin_amdgcn_permlane32_swap(__float_as_uint(x), __float_as_uint(x), false, false);
  return __uint_as_float(r[0]) + __uint_as_float(r[1]);
}
__device__ __forceinline__ unsigned next_tag(unsigned e) { return e * 1664525u + 1013904223u; }
__device__ __forceinline__ int mbcnt64(u64 m, int base) {
  return (int)__builtin_amdgcn_mbcnt_hi((unsigned)(m >> 32), __builtin_amdgcn_mbcnt_lo((unsigned)m, (unsigned)base));
}
template <int CTRL, int RM> __device__ __forceinline__ int dpp0(int x) { return __builtin_amdgcn_update_dpp(0, x, CTRL, RM, 0xf, false); }
template <int CTRL, int RM> __device__ __forceinline__ float dppf(float old, float x) {
  return __int_as_float(__builtin_amdgcn_update_dpp(__float_as_int(old), __float_as_int(x), CTRL, RM, 0xf, false));
}

__device__ __forceinline__ void dma_page(unsigned ldsaddr, u32x4 srd, unsigned soff, unsigned v0, unsigned v1, unsigned v2, unsigned v3) {
  asm volatile(
      "s_nop 4\n\t"
      "s_mov_b32 m0, %0\n\t"
      "s_nop 0\n\t"
      "buffer_load_dwordx4 %3, %1, %2 offen nt lds\n\t"
      "buffer_load_dwordx4 %4, %1, %2 offen offset:1024 nt lds\n\t"
      "buffer_load_dwordx4 %5, %1, %2 offen offset:2048 nt lds\n\t"
      "buffer_load_dwordx4 %6, %1, %2 offen offset:3072 nt lds"
      :: "s"(ldsaddr), "s"(srd), "s"(soff), "v"(v0), "v"(v1), "v"(v2), "v"(v3) : "memory");
}
#define WAITVM(n) asm volatile("s_waitcnt vmcnt(" #n ")" ::: "memory")
__device__ __forceinline__ void wait_pages(int np) {
  if (np >= 2) WAITVM(8); else if (np == 1) WAITVM(4); else WAITVM(0);
}

#define STAMP(k) do {} while (0)

__global__ void __launch_bounds__(NTHR, 2)
attn_main(const void* q_, const void* kc_, const void* vc_, const void* bt_, const void* ctx_, void* ws_, void* out_, unsigned kv_bytes_, unsigned pad_) {
  __shared__ __attribute__((aligned(16))) char smem[LDS_BYTES];
  const int tid = threadIdx.x, lane = tid & 63;
  const int wave = __builtin_amdgcn_readfirstlane(tid >> 6);
  const int bid = blockIdx.x;
  const int col = lane & 31, hh = lane >> 5;
  STAMP(0);
  u32x16 ka = *(const __attribute__((address_space(4))) u32x16*)__builtin_amdgcn_kernarg_segment_ptr();

  int cls = 0, first = 0;
  {
    int c = 0;
#pragma unroll
    for (int k = 0; k < 7; ++k) {
      c += (k == 1 || k == 5) ? NS_B : NS_A;
      if (bid >= c) { cls = k + 1; first = c; }
    }
  }
  asm volatile("" : "+s"(ka), "+s"(cls), "+s"(first));
  STAMP(7);
  const u64 qb = ((u64)ka[1] << 32) | ka[0], btb = ((u64)ka[7] << 32) | ka[6], cb = ((u64)ka[9] << 32) | ka[8];
  const u64 wsb = ((u64)ka[11] << 32) | ka[10], ob = ((u64)ka[13] << 32) | ka[12];

  int id[NBATCH];
  i32x8 cl;
  asm volatile(
      "global_load_dword %0, %9, %11 offset:-4096\n\t"
      "global_load_dword %1, %9, %11 offset:-2048\n\t"
      "global_load_dword %2, %9, %11\n\t"
      "global_load_dword %3, %9, %11 offset:2048\n\t"
      "global_load_dword %4, %10, %11 offset:-4096\n\t"
      "global_load_dword %5, %10, %11 offset:-2048\n\t"
      "global_load_dword %6, %10, %11\n\t"
      "global_load_dword %7, %10, %11 offset:2048\n\t"
      "s_load_dwordx8 %8, %12, 0x0"
      : "=&v"(id[0]), "=&v"(id[1]), "=&v"(id[2]), "=&v"(id[3]), "=&v"(id[4]), "=&v"(id[5]), "=&v"(id[6]), "=&v"(id[7]), "=&s"(cl)
      : "v"(tid * 4 + 4096), "v"(tid * 4 + 12288), "s"(btb), "s"(cb) : "memory");

  const bool weighted = (((ka[2] ^ ka[4]) & 0x7ffu) | (ka[2] & 0xffu)) == 0u;
  const int rot = weighted ? (int)(ka[2] >> 8) & 7 : 0;
  const int head = weighted ? (cls - rot) & 7 : bid >> 5;
  int slot = weighted ? bid - first : bid & 31;
  unsigned magic = !weighted ? 0x08000000u : (cls & 3) == 1 ? (unsigned)(0xffffffffu / NS_B + 1u) : cls == 7 ? (unsigned)(0xffffffffu / NS_C + 1u) : (unsigned)(0xffffffffu / NS_A + 1u);
  const int ccls = ((bid >> 5) + rot) & 7;
  const int nsc = !weighted ? 32 : (ccls & 3) == 1 ? NS_B : ccls == 7 ? NS_C : NS_A;

  bf16x8 qf[8];
  unsigned ep; int myc;
  asm volatile(
      "global_load_dwordx4 %0, %10, %11\n\t"
      "global_load_dwordx4 %1, %10, %11 offset:32\n\t"
      "global_load_dwordx4 %2, %10, %11 offset:64\n\t"
      "global_load_dwordx4 %3, %10, %11 offset:96\n\t"
      "global_load_dwordx4 %4, %10, %11 offset:128\n\t"
      "global_load_dwordx4 %5, %10, %11 offset:160\n\t"
      "global_load_dwordx4 %6, %10, %11 offset:192\n\t"
      "global_load_dwordx4 %7, %10, %11 offset:224\n\t"
      "global_load_dword %8, %12, %13 sc1\n\t"
      "global_load_dword %9, %14, %15"
      : "=&v"(qf[0]), "=&v"(qf[1]), "=&v"(qf[2]), "=&v"(qf[3]), "=&v"(qf[4]), "=&v"(qf[5]), "=&v"(qf[6]), "=&v"(qf[7]), "=&v"(ep), "=&v"(myc)
      : "v"(((col >> 2) * NH + (col & 3)) * (DH * 2) + 16 * hh), "s"(qb + (u64)(head * 4 * DH * 2)),
        "v"((hh ? bid : head * 32 + col) * 4), "s"(wsb), "v"(col & 28), "s"(cb) : "memory");

  u32x4 srdK = {ka[2], ka[3] & 0xffffu, ka[14], 0x00020000u};
  u32x4 srdV = {ka[4], ka[5] & 0xffffu, ka[14], 0x00020000u};
  const unsigned g4 = lane >> 4, p16 = lane & 15;
  unsigned vk[4], vv[4];
#pragma unroll
  for (int i = 0; i < 4; ++i) {
    vk[i] = (4 * i + g4) * TOK_BYTES + 16 * (p16 ^ (4 * i + g4)) - 1024 * i;
    vv[i] = (4 * i + g4) * TOK_BYTES + 16 * (p16 ^ (g4 << 2)) - 1024 * i;
  }
  unsigned t16 = tid * PAGE, cnta = L_CNT + (lane & 7) * 8 + (lane >> 3), enta = L_LIST + 8 * (wave + NW * min(lane, MAXU / (2 * NW) - 1));
  unsigned taba = L_TAB + tid * 32;
  asm volatile("" : "+v"(vk[0]), "+v"(vk[1]), "+v"(vk[2]), "+v"(vk[3]), "+v"(vv[0]), "+v"(vv[1]), "+v"(vv[2]), "+v"(vv[3]),
                    "+v"(t16), "+v"(cnta), "+v"(enta), "+v"(taba));
  asm volatile("s_waitcnt lgkmcnt(0)" : "+s"(cl), "+s"(srdK), "+s"(srdV), "+s"(slot), "+s"(magic) :: "memory");
  u64 live[NBATCH];
#pragma unroll
  for (int b = 0; b < NBATCH; ++b) live[b] = __builtin_amdgcn_ballot_w64(t16 <= (unsigned)cl[b] - 1u);
  asm volatile("" : "+s"(live[0]), "+s"(live[1]), "+s"(live[2]), "+s"(live[3]), "+s"(live[4]), "+s"(live[5]), "+s"(live[6]), "+s"(live[7]));
  asm volatile("s_waitcnt vmcnt(10)"
               : "+v"(id[0]), "+v"(id[1]), "+v"(id[2]), "+v"(id[3]), "+v"(id[4]), "+v"(id[5]), "+v"(id[6]), "+v"(id[7]) :: "memory");
  STAMP(1);

  u64 lead[NBATCH];
#pragma unroll
  for (int b = 0; b < NBATCH; ++b) id[b] = __builtin_amdgcn_inverse_ballot_w64(live[b]) ? id[b] : -1 - b;
  *reinterpret_cast<int4*>(smem + taba) = make_int4(id[0], id[1], id[2], id[3]);
  *reinterpret_cast<int4*>(smem + taba + 16) = make_int4(id[4], id[5], id[6], id[7]);
  {
    unsigned pk[2] = {0u, 0u};
#pragma unroll
    for (int b = 0; b < NBATCH; ++b) {
      u64 dup = 0;
#pragma unroll
      for (int e = 0; e < b; ++e) dup |= __builtin_amdgcn_ballot_w64(id[e] == id[b]);
      lead[b] = live[b] & ~dup;
      pk[b >> 2] |= (unsigned)__builtin_popcountll(lead[b]) << (8 * (b & 3));
    }
    if (lane == 0) *reinterpret_cast<uint2*>(smem + L_CNT + 8 * wave) = make_uint2(pk[0], pk[1]);
  }
  asm volatile("s_waitcnt lgkmcnt(0)\n\ts_barrier"
               : "+v"(id[0]), "+v"(id[1]), "+v"(id[2]), "+v"(id[3]), "+v"(id[4]), "+v"(id[5]), "+v"(id[6]), "+v"(id[7]) :: "memory");
  int lo, n, U, excl;
  {
    const int own = *reinterpret_cast<const unsigned char*>(smem + cnta);
    int x = own;
    x += dpp0<0x111, 0xf>(x); x += dpp0<0x112, 0xf>(x); x += dpp0<0x114, 0xf>(x); x += dpp0<0x118, 0xf>(x);
    x += dpp0<0x142, 0xa>(x); x += dpp0<0x143, 0xc>(x);
    excl = x - own;
    U = __builtin_amdgcn_readlane(x, 63);
    lo = (int)__umulhi((unsigned)(slot * U), magic);
    const int hi = (int)__umulhi((unsigned)((slot + 1) * U), magic);
    n = hi - lo;
    const u64 hit = __builtin_amdgcn_ballot_w64(excl < hi && x > lo) >> wave;
#pragma unroll
    for (int b = 0; b < NBATCH; ++b) {
      if ((hit >> (8 * b)) & 1) {
        const int u = mbcnt64(lead[b], __builtin_amdgcn_readlane(excl, b * 8 + wave) - lo);
        if (__builtin_amdgcn_inverse_ballot_w64(lead[b]) && (unsigned)u < (unsigned)n)
          *reinterpret_cast<unsigned*>(smem + L_LIST + 4 * u) = ((unsigned)id[b] << 9) | (unsigned)tid;
      }
    }
  }
  asm volatile("s_waitcnt lgkmcnt(0)\n\ts_barrier" ::: "memory");
  const int ntiles = (n + 1) >> 1;
  const int nt = (ntiles > wave) ? (ntiles - wave + NW - 1) / NW : 0;
  const bool lastodd = (n & 1) && (nt > 0) && (wave + NW * (nt - 1) == ntiles - 1);
  const uint2 ent = *reinterpret_cast<const uint2*>(smem + enta);

  const unsigned ring = (unsigned)(size_t)smem + wave * 16384;
  const unsigned hoff = head * (DH * 2);
  auto issue = [&](int i, const u32x4& srd, unsigned dst, const unsigned (&vo)[4]) {
    const unsigned pa = ((unsigned)__builtin_amdgcn_readlane(ent.x, i) >> 9) * PAGE_BYTES + hoff;
    dma_page(dst, srd, pa, vo[0], vo[1], vo[2], vo[3]);
    if (!(lastodd && i == nt - 1)) {
      const unsigned pb = ((unsigned)__builtin_amdgcn_readlane(ent.y, i) >> 9) * PAGE_BYTES + hoff;
      dma_page(dst + 4096, srd, pb, vo[0], vo[1], vo[2], vo[3]);
    }
  };
  auto prio = [&](int left) {
    if (PRIO) {
      if (left >= 3) __builtin_amdgcn_s_setprio(3); else if (left == 2) __builtin_amdgcn_s_setprio(2);
      else if (left == 1) __builtin_amdgcn_s_setprio(1); else __builtin_amdgcn_s_setprio(0);
    }
  };
  STAMP(2);
  const int tot = 2 * nt - (lastodd ? 1 : 0);
  if (nt > 0) { prio(nt - 1); issue(0, srdK, ring, vk); issue(0, srdV, ring + 8192, vv); }

  {
    const int bc = (bid & 31) >> 2;
    int idc = id[0];
#pragma unroll
    for (int b = 1; b < NBATCH; ++b) if (bc == b) idc = id[b];
    int u = -1;
#pragma unroll
    for (int b = 0; b < NBATCH; ++b) {
      const int ub = mbcnt64(lead[b], __builtin_amdgcn_readlane(excl, b * 8 + wave));
      if (__builtin_amdgcn_inverse_ballot_w64(lead[b]) && id[b] == idc) u = ub;
    }
    const int s = (int)(((float)((u + 1) * nsc - 1) + 0.5f) * __builtin_amdgcn_rcpf((float)U));
    u64 rest = __builtin_amdgcn_ballot_w64(u >= 0), cm = 0;
    while (rest) {
      const int s0 = __builtin_amdgcn_readlane(s, __builtin_ctzll(rest));
      cm |= 1ull << s0;
      rest &= ~__builtin_amdgcn_ballot_w64(s == s0);
    }
    if (lane == 0) *reinterpret_cast<u64*>(smem + L_CM + 8 * wave) = cm;
  }
  if (tot >= 2) WAITVM(16); else if (tot == 1) WAITVM(8); else WAITVM(0);
  asm volatile("" : "+v"(qf[0]), "+v"(qf[1]), "+v"(qf[2]), "+v"(qf[3]), "+v"(qf[4]), "+v"(qf[5]), "+v"(qf[6]), "+v"(qf[7]), "+v"(ep), "+v"(myc));
  const bool myzero = myc <= 0;
  myc = myzero ? NBLK * PAGE : min(myc, NBLK * PAGE);
  if (myzero) {
#pragma unroll
    for (int s = 0; s < 8; ++s) qf[s] = bf16x8{};
  }

  char* rb = smem + wave * 16384;
  const unsigned krow = col * 256;
  const unsigned tq = (lane & 15) >> 2, tp = lane & 3, blk = (lane >> 4) & 1;
  const unsigned vrow = 8192 + (4 * hh + tq) * 256 + 8 * (tp & 1);

  f32x16 acc[4];
#pragma unroll
  for (int c = 0; c < 4; ++c) acc[c] = f32x16{};
  float m = -INFINITY, lsum = 0.f;
  unsigned wm = 0;

  for (int i = 0; i < nt; ++i) {
    const int npi = min(tot - 2 * i, 2);
    const int npn = max(0, min(tot - 2 * (i + 1), 2));
    const unsigned ea = __builtin_amdgcn_readlane(ent.x, i), eb = __builtin_amdgcn_readlane(ent.y, i);
    const unsigned ta = *reinterpret_cast<const unsigned*>(smem + L_TAB + (ea & 511u) * 32 + (col & 28));
    const unsigned tb = *reinterpret_cast<const unsigned*>(smem + L_TAB + (eb & 511u) * 32 + (col & 28));
    wait_pages(npi);
    if (i == 0) STAMP(3);
    bf16x8 kf[8];
#pragma unroll
    for (int s = 0; s < 8; ++s) kf[s] = *reinterpret_cast<const bf16x8*>(rb + krow + 16 * ((2 * s + hh) ^ (col & 15)));
    asm volatile("s_waitcnt lgkmcnt(0)" ::: "memory");
    if (i + 1 < nt) { prio(nt - 2 - i); issue(i + 1, srdK, ring, vk); }
    f32x16 sc = f32x16{};
#pragma unroll
    for (int s = 0; s < 8; ++s) sc = __builtin_amdgcn_mfma_f32_32x32x16_bf16(kf[s], qf[s], sc, 0, 0, 0);
    const bool ina = ta == (ea >> 9), inb = npi == 2 && tb == (eb >> 9);
    wm |= (unsigned)__builtin_amdgcn_ballot_w64(ina || inb);
    const int nva = (ina ? myc - (int)(ea & 511u) * PAGE : 0) - 4 * hh;
    const int nvb = (inb ? myc - (int)(eb & 511u) * PAGE : 0) - 4 * hh;
    float mx = -INFINITY;
#pragma unroll
    for (int r = 0; r < 16; ++r) {
      const int t = (r & 3) + 8 * ((r >> 2) & 1);
      sc[r] = (t < (r < 8 ? nva : nvb)) ? sc[r] : -INFINITY;
      mx = fmaxf(mx, sc[r]);
    }
    mx = swap32_max(mx);
    const float mn = fmaxf(m, mx);
    const float msub = (mn == -INFINITY) ? 0.f : mn * SCALE_LOG2E;
    const float alpha = __builtin_amdgcn_exp2f(m * SCALE_LOG2E - msub);
    m = mn;
    float ps = 0.f;
#pragma unroll
    for (int r = 0; r < 16; ++r) { sc[r] = __builtin_amdgcn_exp2f(fmaf(sc[r], SCALE_LOG2E, -msub)); ps += sc[r]; }
    lsum = fmaf(lsum, alpha, ps);
    bf16x8 pb[2];
#pragma unroll
    for (int s = 0; s < 2; ++s)
#pragma unroll
      for (int k = 0; k < 4; ++k) {
        const bf16x2 t = __builtin_convertvector((f32x2{sc[8 * s + 2 * k], sc[8 * s + 2 * k + 1]}), bf16x2);
        pb[s][2 * k] = t[0]; pb[s][2 * k + 1] = t[1];
      }
#pragma unroll
    for (int c = 0; c < 4; ++c) acc[c] *= alpha;
    wait_pages(npn);
    bf16x8 vf[2][4];
#pragma unroll
    for (int s = 0; s < 2; ++s)
#pragma unroll
      for (int c = 0; c < 4; ++c) {
        char* a = rb + vrow + 16 * ((4 * c + 2 * blk + (tp >> 1)) ^ (tq << 2)) + s * 4096;
        const bf16x4 x = __builtin_amdgcn_ds_read_tr16_b64_v4bf16((lds_bf16x4_p)(a));
        const bf16x4 y = __builtin_amdgcn_ds_read_tr16_b64_v4bf16((lds_bf16x4_p)(a + 2048));
        vf[s][c] = __builtin_shufflevector(x, y, 0, 1, 2, 3, 4, 5, 6, 7);
      }
    asm volatile("s_waitcnt lgkmcnt(0)" ::: "memory");
    if (i + 1 < nt) issue(i + 1, srdV, ring + 8192, vv);
#pragma unroll
    for (int c = 0; c < 4; ++c) acc[c] = __builtin_amdgcn_mfma_f32_32x32x16_bf16(vf[0][c], pb[0], acc[c], 0, 0, 0);
    if (npi == 2) {
#pragma unroll
      for (int c = 0; c < 4; ++c) acc[c] = __builtin_amdgcn_mfma_f32_32x32x16_bf16(vf[1][c], pb[1], acc[c], 0, 0, 0);
    }
  }
  STAMP(4);

  {
    const float ltot = swap32_sum(lsum);
    if (lane < 32) *reinterpret_cast<f32x2*>(smem + L_ML + (wave * 32 + lane) * 8) = f32x2{m * SCALE_LOG2E, ltot};
    if (lane == 0) *reinterpret_cast<unsigned*>(smem + L_WM + 4 * wave) = wm;
    if ((wm >> col) & 1u) {
#pragma unroll
      for (int c = 0; c < 4; ++c)
#pragma unroll
        for (int g = 0; g < 4; ++g)
          *reinterpret_cast<f32x4*>(rb + col * 512 + 16 * ((8 * c + 2 * g + hh) ^ col)) = f32x4{acc[c][4 * g], acc[c][4 * g + 1], acc[c][4 * g + 2], acc[c][4 * g + 3]};
    }
  }
  asm volatile("s_waitcnt lgkmcnt(0)\n\ts_barrier" ::: "memory");
  const __amdgpu_buffer_rsrc_t wsr = __builtin_amdgcn_make_buffer_rsrc((void*)wsb, 0, WS_BYTES, 0x00020000);
  const unsigned wmv = *reinterpret_cast<const unsigned*>(smem + L_WM + 4 * (lane & 7));
  const u64 cmv = *reinterpret_cast<const u64*>(smem + L_CM + 8 * (lane & 7));
  unsigned wms[NW], rowmask = 0;
#pragma unroll
  for (int w = 0; w < NW; ++w) { wms[w] = __builtin_amdgcn_readlane(wmv, w); rowmask |= wms[w]; }
#pragma unroll
  for (int it = 0; it < 4; ++it) {
    const int row = wave + 8 * it;
    if ((rowmask >> row) & 1u) {
      f32x2 ml[NW], o[NW];
#pragma unroll
      for (int w = 0; w < NW; ++w) {
        ml[w] = *reinterpret_cast<const f32x2*>(smem + L_ML + (w * 32 + row) * 8);
        o[w] = *reinterpret_cast<const f32x2*>(smem + w * 16384 + row * 512 + 16 * ((lane >> 1) ^ row) + 8 * (lane & 1));
      }
      float M = -INFINITY;
#pragma unroll
      for (int w = 0; w < NW; ++w) M = fmaxf(M, ml[w][0]);
      const float Ms = (M == -INFINITY) ? 0.f : M;
      float L = 0.f; f32x2 a = f32x2{0.f, 0.f};
#pragma unroll
      for (int w = 0; w < NW; ++w) {
        const float f = __builtin_amdgcn_exp2f(ml[w][0] - Ms);
        L = fmaf(ml[w][1], f, L);
        if ((wms[w] >> row) & 1u) a += o[w] * f;
      }
      const unsigned tag = next_tag(__builtin_amdgcn_readlane(ep, row));
      const unsigned so = WS_REC + ((head * 32 + row) * MAXS + slot) * REC;
      __builtin_amdgcn_raw_buffer_store_b128(u32x4{__float_as_uint(a[0]), tag, __float_as_uint(a[1]), tag}, wsr, lane * 16, so, 16);
      if (lane == 0) __builtin_amdgcn_raw_buffer_store_b128(u32x4{__float_as_uint(M), tag, __float_as_uint(L), tag}, wsr, 1024, so, 16);
    }
  }
  STAMP(5);

  {
    unsigned clo = 0, chi = 0;
#pragma unroll
    for (int w = 0; w < NW; ++w) { clo |= __builtin_amdgcn_readlane((unsigned)cmv, w); chi |= __builtin_amdgcn_readlane((unsigned)(cmv >> 32), w); }
    const u64 cm = ((u64)chi << 32) | clo;
    u64 my = cm & __builtin_amdgcn_ballot_w64((mbcnt64(cm, 0) & 7) == wave);
    const unsigned tag = next_tag(__builtin_amdgcn_readlane(ep, 32));
    const unsigned rbase = WS_REC + bid * MAXS * REC;
    float M = -INFINITY, L = 0.f, a0 = 0.f, a1 = 0.f;
    while (my) {
      const unsigned sa = rbase + __builtin_ctzll(my) * REC;
      my &= my - 1;
      const bool two = my != 0;
      const unsigned sb = two ? rbase + __builtin_ctzll(my) * REC : sa;
      my &= my - 1;
      u32x4 va, vb, xa, xb;
#pragma nounroll
      for (int spin = 0; spin < (1 << 18); ++spin) {
        asm volatile("" ::: "memory");
        va = __builtin_amdgcn_raw_buffer_load_b128(wsr, lane * 16, sa, 16);
        xa = __builtin_amdgcn_raw_buffer_load_b128(wsr, 1024, sa, 16);
        vb = __builtin_amdgcn_raw_buffer_load_b128(wsr, lane * 16, sb, 16);
        xb = __builtin_amdgcn_raw_buffer_load_b128(wsr, 1024, sb, 16);
#pragma unroll
        for (int k = 0; k < 4; ++k) { xa[k] = __builtin_amdgcn_readfirstlane(xa[k]); xb[k] = __builtin_amdgcn_readfirstlane(xb[k]); }
        const unsigned bad = ((va[1] ^ tag) | (va[3] ^ tag) | (xa[1] ^ tag)) | ((xa[3] ^ tag) | (vb[1] ^ tag) | (vb[3] ^ tag)) | ((xb[1] ^ tag) | (xb[3] ^ tag));
        if (__builtin_amdgcn_ballot_w64(bad != 0u) == 0) break;
        __builtin_amdgcn_s_sleep(1);
      }
      const float ma = __uint_as_float(xa[0]), mb = two ? __uint_as_float(xb[0]) : -INFINITY;
      const float Mn = fmaxf(M, fmaxf(ma, mb));
      const float Ms = (Mn == -INFINITY) ? 0.f : Mn;
      const float al = __builtin_amdgcn_exp2f(M - Ms), fa = __builtin_amdgcn_exp2f(ma - Ms), fb = __builtin_amdgcn_exp2f(mb - Ms);
      M = Mn;
      L = fmaf(__uint_as_float(xb[2]), fb, fmaf(__uint_as_float(xa[2]), fa, L * al));
      a0 = fmaf(__uint_as_float(vb[0]), fb, fmaf(__uint_as_float(va[0]), fa, a0 * al));
      a1 = fmaf(__uint_as_float(vb[2]), fb, fmaf(__uint_as_float(va[2]), fa, a1 * al));
    }
    *reinterpret_cast<f32x4*>(smem + L_TAB + wave * 1024 + lane * 16) = f32x4{a0, a1, M, L};
    asm volatile("s_waitcnt lgkmcnt(0)\n\ts_barrier" ::: "memory");
    if (wave == 0) {
      f32x4 p[NW];
#pragma unroll
      for (int w = 0; w < NW; ++w) p[w] = *reinterpret_cast<const f32x4*>(smem + L_TAB + w * 1024 + lane * 16);
      float Mx = -INFINITY;
#pragma unroll
      for (int w = 0; w < NW; ++w) Mx = fmaxf(Mx, p[w][2]);
      const float Ms = (Mx == -INFINITY) ? 0.f : Mx;
      float o0 = 0.f, o1 = 0.f, Ls = 0.f;
#pragma unroll
      for (int w = 0; w < NW; ++w) {
        const float f = __builtin_amdgcn_exp2f(p[w][2] - Ms);
        o0 = fmaf(p[w][0], f, o0); o1 = fmaf(p[w][1], f, o1); Ls = fmaf(p[w][3], f, Ls);
      }
      const float inv = __builtin_amdgcn_rcpf(Ls);
      const bf16x2 r = __builtin_convertvector((f32x2{o0 * inv, o1 * inv}), bf16x2);
      const int c = bid & 31;
      *reinterpret_cast<__attribute__((address_space(1))) bf16x2*>(ob + (u64)((((c >> 2) * NH + (bid >> 5) * 4 + (c & 3)) * DH + 2 * lane) * 2)) = r;
      if (lane == 0) __builtin_amdgcn_raw_buffer_store_b32(tag, wsr, bid * 4, 0, 16);
    }
  }
}

extern "C" void kernel_launch(void* const* d_in, const int* in_sizes, int n_in, void* d_out, int out_size, void* d_ws, size_t ws_size,
                              hipStream_t stream) {
  const unsigned kv_bytes = (unsigned)in_sizes[1] * 2u;
  attn_main<<<256, NTHR, 0, stream>>>(d_in[0], d_in[1], d_in[2], d_in[3], d_in[4], d_ws, d_out, kv_bytes, 0u);
}
```

```cpp
#include <hip/hip_runtime.h>
#include <hip/hip_bf16.h>
#include <stdint.h>

#define NW 8
#define PRIO 1
#define GAP 9
#define PACE 8
#define PACE_T 3
#define TOUCH 0
#define NG 6
#define NS_A 29
#define NS_B 39
#define NS_C 33

typedef __attribute__((ext_vector_type(8))) __bf16 bf16x8;
typedef __attribute__((ext_vector_type(4))) __bf16 bf16x4;
typedef __attribute__((ext_vector_type(2))) __bf16 bf16x2;
typedef __attribute__((ext_vector_type(2))) float f32x2;
typedef __attribute__((ext_vector_type(4))) float f32x4;
typedef __attribute__((ext_vector_type(16))) float f32x16;
typedef __attribute__((ext_vector_type(2))) unsigned u32x2;
typedef __attribute__((ext_vector_type(4))) unsigned u32x4;
typedef __attribute__((ext_vector_type(8))) int i32x8;
typedef __attribute__((ext_vector_type(8))) unsigned u32x8;
typedef __attribute__((ext_vector_type(32))) unsigned u32x32;
typedef __attribute__((ext_vector_type(16))) unsigned u32x16;
typedef __attribute__((address_space(3))) bf16x4* lds_bf16x4_p;
typedef unsigned long long u64;
template <int N> struct IC { static constexpr int value = N; };

constexpr int NBATCH = 8, NH = 32, NHKV = 8, DH = 128, PAGE = 16, NBLK = 512;
constexpr int NTHR = NW * 64;
static_assert(5 * NS_A + 2 * NS_B + NS_C == 256, "grid");
constexpr int MAXS = 48;
constexpr int MAXU = 160;
static_assert(NS_A >= 16 && NS_B >= NS_A && NS_C >= NS_A && NS_B <= MAXS && NS_C <= MAXS && 4096 / NS_A + 2 <= MAXU, "limits");
constexpr int PAGE_BYTES = PAGE * NHKV * DH * 2;
constexpr int TOK_BYTES = NHKV * DH * 2;
constexpr float SCALE_LOG2E = 0.08838834764831845f * 1.4426950408889634f;
constexpr float LOWEST = -3.0e38f;

constexpr int L_TAB = NW * 16384;
constexpr int L_Q = L_TAB + NBLK * NBATCH * 4;
constexpr int L_CNT = L_Q + 32 * DH * 2;
constexpr int L_CM = L_CNT + 64;
constexpr int L_CL = L_CM + 64;
constexpr int L_LIST = L_CL + 256;
constexpr int L_ML = L_LIST + MAXU * 4;
constexpr int LDS_BYTES = L_ML + NW * 264;
static_assert(LDS_BYTES <= 163840 && (L_LIST & 15) == 0, "lds");

constexpr unsigned WS_HEAD = 1u << 21;
constexpr unsigned WS_REC = 1024;
constexpr unsigned REC = 1024;
static_assert(WS_REC + 32u * MAXS * REC <= WS_HEAD, "ws");

__device__ __forceinline__ float swap32_max(float x) {
  auto r = __builtin_amdgcn_permlane32_swap(__float_as_uint(x), __float_as_uint(x), false, false);
  return fmaxf(__uint_as_float(r[0]), __uint_as_float(r[1]));
}
__device__ __forceinline__ float swap32_sum(float x) {
  auto r = __builtin_amdgcn_permlane32_swap(__float_as_uint(x), __float_as_uint(x), false, false);
  return __uint_as_float(r[0]) + __uint_as_float(r[1]);
}
__device__ __forceinline__ float swap16_max(float x) {
  auto r = __builtin_amdgcn_permlane16_swap(__float_as_uint(x), __float_as_uint(x), false, false);
  return fmaxf(__uint_as_float(r[0]), __uint_as_float(r[1]));
}
__device__ __forceinline__ float swap16_sum(float x) {
  auto r = __builtin_amdgcn_permlane16_swap(__float_as_uint(x), __float_as_uint(x), false, false);
  return __uint_as_float(r[0]) + __uint_as_float(r[1]);
}
__device__ __forceinline__ unsigned next_tag(unsigned e) { return e * 1664525u + 1013904223u; }
__device__ __forceinline__ int mbcnt64(u64 m, int base) {
  return (int)__builtin_amdgcn_mbcnt_hi((unsigned)(m >> 32), __builtin_amdgcn_mbcnt_lo((unsigned)m, (unsigned)base));
}
template <int CTRL, int RM> __device__ __forceinline__ int dpp0(int x) { return __builtin_amdgcn_update_dpp(0, x, CTRL, RM, 0xf, false); }
template <int CTRL> __device__ __forceinline__ float dppf(float x) {
  return __int_as_float(__builtin_amdgcn_mov_dpp(__float_as_int(x), CTRL, 0xf, 0xf, true));
}

__device__ __forceinline__ void put2(u32x4 a, u32x4 b, unsigned voff, u32x4 srd, unsigned soff) {
  asm volatile("buffer_store_dwordx4 %0, %2, %3, %4 offen sc1\n\tbuffer_store_dwordx4 %1, %2, %3, %4 offen offset:512 sc1\n\ts_nop 1"
               :: "v"(a), "v"(b), "v"(voff), "s"(srd), "s"(soff) : "memory");
}
__device__ __forceinline__ void dma_page(unsigned ldsaddr, u32x4 srd, unsigned soff, unsigned v0, unsigned v1, unsigned v2, unsigned v3) {
  asm volatile(
      "s_nop 4\n\t"
      "s_mov_b32 m0, %0\n\t"
      "s_nop 0\n\t"
      "buffer_load_dwordx4 %3, %1, %2 offen nt lds\n\t"
      "buffer_load_dwordx4 %4, %1, %2 offen offset:1024 nt lds\n\t"
      "buffer_load_dwordx4 %5, %1, %2 offen offset:2048 nt lds\n\t"
      "buffer_load_dwordx4 %6, %1, %2 offen offset:3072 nt lds"
      :: "s"(ldsaddr), "s"(srd), "s"(soff), "v"(v0), "v"(v1), "v"(v2), "v"(v3) : "memory");
}
#define WAITVM(n) asm volatile("s_waitcnt vmcnt(" #n ")" ::: "memory")
__device__ __forceinline__ void wait_pages(int np) {
  if (np >= 2) WAITVM(8); else if (np == 1) WAITVM(4); else WAITVM(0);
}

#define STAMP(k) do {} while (0)

__global__ void __launch_bounds__(NTHR, 2)
attn_main(const void* q_, const void* kc_, const void* vc_, const void* bt_, const void* ctx_, void* ws_, void* out_, unsigned kv_bytes_, unsigned pad_) {
  __shared__ __attribute__((aligned(16))) char smem[LDS_BYTES];
  const int tid = threadIdx.x, lane = tid & 63;
  const int wave = __builtin_amdgcn_readfirstlane(tid >> 6);
  const int bid = blockIdx.x;
  const int col = lane & 31, hh = lane >> 5;
  STAMP(0);
  u32x16 ka;
  asm volatile("s_load_dwordx16 %0, %1, 0x0\n\ts_setprio 3" : "=&s"(ka) : "s"(__builtin_amdgcn_kernarg_segment_ptr()));

  constexpr int HALF = 3 * NS_A + NS_B;
  const bool up = bid >= HALF;
  const int rel = up ? bid - HALF : bid;
  const bool c1 = rel >= NS_A, c2 = rel >= NS_A + NS_B, c3 = rel >= 2 * NS_A + NS_B;
  int cls = (up ? 4 : 0) + (c3 ? 3 : c2 ? 2 : c1 ? 1 : 0);
  int slot = rel - (c3 ? 2 * NS_A + NS_B : c2 ? NS_A + NS_B : c1 ? NS_A : 0);
  int ns = cls == 7 ? NS_C : (cls & 3) == 1 ? NS_B : NS_A;
  unsigned magic = cls == 7 ? 0xffffffffu / NS_C + 1u : (cls & 3) == 1 ? 0xffffffffu / NS_B + 1u : 0xffffffffu / NS_A + 1u;
  int be = (int)__umulhi((unsigned)(slot * NW + wave), magic);
  unsigned ring = (unsigned)(size_t)smem + wave * 16384;
  unsigned rowa = be * (NBLK * 4) + lane * 16, taa = tid * 4 + 4096, tab2 = tid * 4 + 12288;
  unsigned be4 = be * 4, rowl = ring + 8192;
  asm volatile("" : "+v"(rowa), "+v"(taa), "+v"(tab2));
  asm volatile("s_waitcnt lgkmcnt(0)" : "+s"(ka), "+s"(cls), "+s"(slot), "+s"(ns), "+s"(magic), "+s"(be), "+s"(ring), "+s"(be4), "+s"(rowl));
  const u64 qb = ((u64)ka[1] << 32) | ka[0], btb = ((u64)ka[7] << 32) | ka[6], cb = ((u64)ka[9] << 32) | ka[8];
  const u64 ws0 = ((u64)ka[11] << 32) | ka[10], ob = ((u64)ka[13] << 32) | ka[12];

  int id[NBATCH];
  i32x8 cl;
  int cle;
  asm volatile(
      "s_mov_b32 m0, %16\n\t"
      "s_load_dword %9, %13, %14\n\t"
      "global_load_lds_dwordx4 %15, %12\n\t"
      "global_load_lds_dwordx4 %15, %12 offset:1024\n\t"
      "s_load_dwordx8 %8, %13, 0x0\n\t"
      "global_load_dword %0, %10, %12 offset:-4096\n\t"
      "global_load_dword %1, %10, %12 offset:-2048\n\t"
      "global_load_dword %2, %10, %12\n\t"
      "global_load_dword %3, %10, %12 offset:2048\n\t"
      "global_load_dword %4, %11, %12 offset:-4096\n\t"
      "global_load_dword %5, %11, %12 offset:-2048\n\t"
      "global_load_dword %6, %11, %12\n\t"
      "global_load_dword %7, %11, %12 offset:2048"
      : "=&v"(id[0]), "=&v"(id[1]), "=&v"(id[2]), "=&v"(id[3]), "=&v"(id[4]), "=&v"(id[5]), "=&v"(id[6]), "=&v"(id[7]), "=&s"(cl), "=&s"(cle)
      : "v"(taa), "v"(tab2), "s"(btb), "s"(cb), "s"(be4), "v"(rowa), "s"(rowl) : "memory");

  int head = (cls - (int)(ka[2] >> 8)) & 7;
  const u64 wsb = ws0 + (u64)(head * WS_HEAD);
  unsigned ep; int myc;
  asm volatile(
      "s_mov_b32 m0, %8\n\t"
      "global_load_dword %0, %4, %5 sc1\n\t"
      "global_load_lds_dwordx4 %2, %3\n\t"
      "global_load_dword %1, %6, %7"
      : "=&v"(ep), "=&v"(myc)
      : "v"((lane & 48) * 16 + 16 * ((lane ^ (4 * wave + (lane >> 4))) & 15)), "s"(qb + (u64)((wave * NH + head * 4) * DH * 2)), "v"(col * 4),
        "s"(wsb), "v"(col & 28), "s"(cb), "s"((unsigned)(size_t)smem + L_Q + wave * 1024) : "memory");
  const unsigned g4 = lane >> 4, p16 = lane & 15;
  unsigned vk[4], vv[4];
#pragma unroll
  for (int i = 0; i < 4; ++i) {
    vk[i] = (4 * i + g4) * TOK_BYTES + 16 * (p16 ^ (4 * i + g4)) - 1024 * i;
    vv[i] = (4 * i + g4) * TOK_BYTES + 16 * (p16 ^ (g4 << 2)) - 1024 * i;
  }
  int je2 = 2 * (slot * NW + wave - be * ns);
  asm volatile("" : "+v"(vk[0]), "+v"(vk[1]), "+v"(vk[2]), "+v"(vk[3]), "+v"(vv[0]), "+v"(vv[1]), "+v"(vv[2]), "+v"(vv[3]));
  asm volatile("" : "+s"(je2));

  u32x4 srdK = {ka[2], ka[3] & 0xffffu, ka[14], 0x00020000u};
  u32x4 srdV = {ka[4], ka[5] & 0xffffu, ka[14], 0x00020000u};
  unsigned hoff = head * (DH * 2);
  asm volatile("s_waitcnt lgkmcnt(0)" : "+s"(cl), "+s"(cle), "+s"(srdK), "+s"(srdV), "+s"(hoff) :: "memory");
  const int colB = (int)(min((unsigned)cle - 1u, (unsigned)(NBLK * PAGE - 1)) >> 4) - je2;
  const int cA = max(colB - 1, 0);
  u32x2 e2;
  asm volatile("s_waitcnt vmcnt(11)\n\tds_read2_b32 %0, %1 offset1:1\n\ts_waitcnt lgkmcnt(0)" : "=v"(e2) : "v"(rowl + 4 * cA) : "memory");
  const unsigned pa0 = (unsigned)__builtin_amdgcn_readfirstlane(e2[0]) * PAGE_BYTES + hoff;
  const unsigned pb0 = (unsigned)__builtin_amdgcn_readfirstlane(e2[1]) * PAGE_BYTES + hoff;
  dma_page(ring, srdK, pa0, vk[0], vk[1], vk[2], vk[3]);
  dma_page(ring + 4096, srdK, pb0, vk[0], vk[1], vk[2], vk[3]);
  __builtin_amdgcn_s_setprio(0);

  u64 live[NBATCH], lead[NBATCH];
  {
    const unsigned t16 = (tid + 2 * ns) * PAGE;
#pragma unroll
    for (int b = 0; b < NBATCH; ++b) live[b] = __builtin_amdgcn_ballot_w64(t16 <= min((unsigned)cl[b] - 1u, (unsigned)(NBLK * PAGE - 1)));
  }
  asm volatile("s_waitcnt vmcnt(11)"
               : "+v"(id[0]), "+v"(id[1]), "+v"(id[2]), "+v"(id[3]), "+v"(id[4]), "+v"(id[5]), "+v"(id[6]), "+v"(id[7]) :: "memory");
#pragma unroll
  for (int b = 0; b < NBATCH; ++b) id[b] = __builtin_amdgcn_inverse_ballot_w64(live[b]) ? id[b] : -1 - b;
  *reinterpret_cast<int4*>(smem + L_TAB + tid * 32) = make_int4(id[0], id[1], id[2], id[3]);
  *reinterpret_cast<int4*>(smem + L_TAB + tid * 32 + 16) = make_int4(id[4], id[5], id[6], id[7]);
  {
    unsigned pk[2] = {0u, 0u};
#pragma unroll
    for (int b = 0; b < NBATCH; ++b) {
      u64 dup = 0;
#pragma unroll
      for (int e = 0; e < b; ++e) dup |= __builtin_amdgcn_ballot_w64(id[e] == id[b]);
      lead[b] = live[b] & ~dup;
      pk[b >> 2] |= (unsigned)__builtin_popcountll(lead[b]) << (8 * (b & 3));
    }
    if (lane == 0) *reinterpret_cast<uint2*>(smem + L_CNT + 8 * wave) = make_uint2(pk[0], pk[1]);
  }
  asm volatile("s_waitcnt vmcnt(8) lgkmcnt(0)\n\ts_barrier" : "+v"(ep), "+v"(myc) :: "memory");
  const int cs = slot - (ns - 16);
  int n;
  {
    const int own = *reinterpret_cast<const unsigned char*>(smem + L_CNT + (lane & 7) * 8 + (lane >> 3));
    int x = own;
    x += dpp0<0x111, 0xf>(x); x += dpp0<0x112, 0xf>(x); x += dpp0<0x114, 0xf>(x); x += dpp0<0x118, 0xf>(x);
    x += dpp0<0x142, 0xa>(x); x += dpp0<0x143, 0xc>(x);
    const int excl = x - own;
    const int U = __builtin_amdgcn_readlane(x, 63);
    const int lo = (int)__umulhi((unsigned)(slot * U), magic);
    n = (int)__umulhi((unsigned)((slot + 1) * U), magic) - lo;
    int idc = id[0];
#pragma unroll
    for (int b = 1; b < NBATCH; ++b) if ((cs >> 1) == b) idc = id[b];
    int uc = -1;
#pragma unroll
    for (int b = 0; b < NBATCH; ++b) {
      const int u = mbcnt64(lead[b], __builtin_amdgcn_readlane(excl, b * 8 + wave));
      const bool ld = __builtin_amdgcn_inverse_ballot_w64(lead[b]);
      if (ld && (unsigned)(u - lo) < (unsigned)n) *reinterpret_cast<unsigned*>(smem + L_LIST + 4 * (u - lo)) = ((unsigned)id[b] << 9) | (unsigned)tid;
      if (ld && id[b] == idc) uc = u;
    }
    if (cs >= 0) {
      const int s = (int)(((float)((uc + 1) * ns - 1) + 0.5f) * __builtin_amdgcn_rcpf((float)U));
      u64 rest = __builtin_amdgcn_ballot_w64(uc >= 0), cm = 0;
      while (rest) {
        const int s0 = __builtin_amdgcn_readlane(s, __builtin_ctzll(rest));
        cm |= 1ull << s0;
        rest &= ~__builtin_amdgcn_ballot_w64(s == s0);
      }
      if (lane == 0) *reinterpret_cast<u64*>(smem + L_CM + 8 * wave) = cm;
    }
  }
  asm volatile("s_waitcnt lgkmcnt(0)\n\ts_barrier" ::: "memory");
  STAMP(7);
  dma_page(ring + 8192, srdV, pa0, vv[0], vv[1], vv[2], vv[3]);
  dma_page(ring + 12288, srdV, pb0, vv[0], vv[1], vv[2], vv[3]);
  const int ntiles = (n + 1) >> 1;
  const int nl = (ntiles > wave) ? (ntiles - wave + NW - 1) / NW : 0;
  const int tot = 2 * nl - (((n & 1) && nl > 0 && wave + NW * (nl - 1) == ntiles - 1) ? 1 : 0);
  const uint2 ent = *reinterpret_cast<const uint2*>(smem + L_LIST + 8 * (wave + NW * min(lane, MAXU / (2 * NW) - 1)));

  bool act0, act1;
  {
    const int b0 = wave >> 1, b1 = b0 + 4;
    const int e0 = (int)__umulhi((unsigned)(slot * NW), magic), e1 = (int)__umulhi((unsigned)(slot * NW + NW - 1), magic);
    u64 h0 = 0, h1 = 0;
    for (int r = 0; r < n; r += 64) {
      const unsigned en = *reinterpret_cast<const unsigned*>(smem + L_LIST + 4 * min(r + lane, n - 1));
      const unsigned t0 = *reinterpret_cast<const unsigned*>(smem + L_TAB + (en & 511u) * 32 + 4 * b0);
      const unsigned t1 = *reinterpret_cast<const unsigned*>(smem + L_TAB + (en & 511u) * 32 + 4 * b0 + 16);
      h0 |= __builtin_amdgcn_ballot_w64(t0 == (en >> 9));
      h1 |= __builtin_amdgcn_ballot_w64(t1 == (en >> 9));
    }
    act0 = h0 != 0 || b0 == e0 || b0 == e1;
    act1 = h1 != 0 || b1 == e0 || b1 == e1;
  }
  int ncon = 0;
  if (cs >= 0) {
    const u64 cmv = *reinterpret_cast<const u64*>(smem + L_CM + 8 * (lane & 7));
    unsigned clo = 0, chi = 0;
#pragma unroll
    for (int w = 0; w < NW; ++w) { clo |= __builtin_amdgcn_readlane((unsigned)cmv, w); chi |= __builtin_amdgcn_readlane((unsigned)(cmv >> 32), w); }
    const int bc = cs >> 1;
    const u64 cm = (((u64)chi << 32) | clo) | __builtin_amdgcn_ballot_w64(lane >= ((bc * ns) >> 3) && lane <= (((bc + 1) * ns - 1) >> 3));
    ncon = __builtin_popcountll(cm);
    if (__builtin_amdgcn_inverse_ballot_w64(cm)) *reinterpret_cast<unsigned*>(smem + L_CL + 4 * mbcnt64(cm, 0)) = lane * REC;
  }

  bf16x8 qf[8];
#pragma unroll
  for (int s = 0; s < 8; ++s) qf[s] = *reinterpret_cast<const bf16x8*>(smem + L_Q + col * 256 + 16 * ((2 * s + hh) ^ (col & 15)));
  const bool myzero = myc <= 0;
  myc = (myzero ? NBLK * PAGE : min(myc, NBLK * PAGE)) - 4 * hh;
  if (myzero) {
#pragma unroll
    for (int s = 0; s < 8; ++s) qf[s] = bf16x8{};
  }
  ep = next_tag(ep);
  int actm = __builtin_amdgcn_readfirstlane((act0 ? 1 : 0) | (act1 ? 2 : 0));
  ncon = __builtin_amdgcn_readfirstlane(ncon);
  asm volatile("" : "+s"(actm), "+s"(ncon));

  auto prio = [&](int left) {
    if (PRIO) {
      if (left >= 3) __builtin_amdgcn_s_setprio(3); else if (left == 2) __builtin_amdgcn_s_setprio(2);
      else if (left == 1) __builtin_amdgcn_s_setprio(1); else __builtin_amdgcn_s_setprio(0);
    }
  };
  prio(nl);

  unsigned ka8[8], va4[4];
  {
    const unsigned tq = (lane & 15) >> 2, tp = lane & 3, blk = (lane >> 4) & 1;
#pragma unroll
    for (int s = 0; s < 8; ++s) ka8[s] = ring + col * 256 + 16 * ((2 * s + hh) ^ (col & 15));
#pragma unroll
    for (int c = 0; c < 4; ++c) va4[c] = ring + 8192 + (4 * hh + tq) * 256 + 8 * (tp & 1) + 16 * ((4 * c + 2 * blk + (tp >> 1)) ^ (tq << 2));
    asm volatile("" : "+v"(ka8[0]), "+v"(ka8[1]), "+v"(ka8[2]), "+v"(ka8[3]), "+v"(ka8[4]), "+v"(ka8[5]), "+v"(ka8[6]), "+v"(ka8[7]),
                      "+v"(va4[0]), "+v"(va4[1]), "+v"(va4[2]), "+v"(va4[3]));
  }

  f32x16 acc[4];
#pragma unroll
  for (int c = 0; c < 4; ++c) acc[c] = f32x16{};
  float ms = LOWEST, lsum = 0.f;
  f32x16 sc;
  auto mask = [&](int nva, int nvb) {
#pragma unroll
    for (int r = 0; r < 16; ++r) sc[r] = ((r & 3) + 8 * ((r >> 2) & 1) < (r < 8 ? nva : nvb)) ? 0.f : -INFINITY;
    asm volatile("" : "+v"(sc));
  };
  mask(((col >> 2) == be && colB >= 0) ? myc - cA * PAGE : 0, ((col >> 2) == be && colB >= 1) ? myc - (cA + 1) * PAGE : 0);
  bf16x8 pf[2];

  auto scores = [&](const bf16x8 (&kf)[8]) {
#pragma unroll
    for (int s = 0; s < 8; ++s) sc = __builtin_amdgcn_mfma_f32_32x32x16_bf16(kf[s], qf[s], sc, 0, 0, 0);
    float mx = -INFINITY;
#pragma unroll
    for (int r = 0; r < 16; ++r) mx = fmaxf(mx, sc[r]);
    mx = swap32_max(mx);
    const float mn = fmaxf(ms, mx * SCALE_LOG2E);
    const float alpha = __builtin_amdgcn_exp2f(ms - mn);
    ms = mn;
    float ps = 0.f;
#pragma unroll
    for (int r = 0; r < 16; ++r) { sc[r] = __builtin_amdgcn_exp2f(fmaf(sc[r], SCALE_LOG2E, -mn)); ps += sc[r]; }
    lsum = fmaf(lsum, alpha, ps);
#pragma unroll
    for (int s = 0; s < 2; ++s)
#pragma unroll
      for (int k = 0; k < 4; ++k) {
        const bf16x2 t = __builtin_convertvector((f32x2{sc[8 * s + 2 * k], sc[8 * s + 2 * k + 1]}), bf16x2);
        pf[s][2 * k] = t[0]; pf[s][2 * k + 1] = t[1];
      }
#pragma unroll
    for (int c = 0; c < 4; ++c) acc[c] *= alpha;
    asm volatile("" : "+v"(acc[0]), "+v"(acc[1]), "+v"(acc[2]), "+v"(acc[3]), "+v"(pf[0]), "+v"(pf[1]));
  };
  auto readk = [&](bf16x8 (&kf)[8]) {
#pragma unroll
    for (int s = 0; s < 8; ++s) kf[s] = *(const __attribute__((address_space(3))) bf16x8*)ka8[s];
  };
  auto readv = [&](bf16x8 (&vf)[4][2]) {
#pragma unroll
    for (int c = 0; c < 4; ++c)
#pragma unroll
      for (int s = 0; s < 2; ++s) {
        const bf16x4 x = __builtin_amdgcn_ds_read_tr16_b64_v4bf16((lds_bf16x4_p)(va4[c] + s * 4096));
        const bf16x4 y = __builtin_amdgcn_ds_read_tr16_b64_v4bf16((lds_bf16x4_p)(va4[c] + s * 4096 + 2048));
        vf[c][s] = __builtin_shufflevector(x, y, 0, 1, 2, 3, 4, 5, 6, 7);
      }
  };

#pragma nounroll
  for (int i = 0; i < nl; ++i) {
    const bool two = tot - 2 * i >= 2;
    const unsigned ea = __builtin_amdgcn_readlane(ent.x, i), eb = __builtin_amdgcn_readlane(ent.y, i);
    const unsigned ta = *reinterpret_cast<const unsigned*>(smem + L_TAB + (ea & 511u) * 32 + (col & 28));
    const unsigned tb = *reinterpret_cast<const unsigned*>(smem + L_TAB + (eb & 511u) * 32 + (col & 28));
    const unsigned pa = (ea >> 9) * PAGE_BYTES + hoff, pb = (eb >> 9) * PAGE_BYTES + hoff;
    WAITVM(8);
    bf16x8 kf[8];
    readk(kf);
    asm volatile("s_waitcnt lgkmcnt(0)" ::: "memory");
    prio(nl - 1 - i);
    dma_page(ring, srdK, pa, vk[0], vk[1], vk[2], vk[3]);
    if (two) dma_page(ring + 4096, srdK, pb, vk[0], vk[1], vk[2], vk[3]);
    scores(kf);
    if (two) WAITVM(8); else WAITVM(4);
    bf16x8 vf[4][2];
    readv(vf);
    asm volatile("s_waitcnt lgkmcnt(0)" ::: "memory");
    dma_page(ring + 8192, srdV, pa, vv[0], vv[1], vv[2], vv[3]);
    if (two) dma_page(ring + 12288, srdV, pb, vv[0], vv[1], vv[2], vv[3]);
#pragma unroll
    for (int c = 0; c < 4; ++c) {
      acc[c] = __builtin_amdgcn_mfma_f32_32x32x16_bf16(vf[c][0], pf[0], acc[c], 0, 0, 0);
      acc[c] = __builtin_amdgcn_mfma_f32_32x32x16_bf16(vf[c][1], pf[1], acc[c], 0, 0, 0);
    }
    mask(ta == (ea >> 9) ? myc - (int)(ea & 511u) * PAGE : 0, (two && tb == (eb >> 9)) ? myc - (int)(eb & 511u) * PAGE : 0);
  }

  const int crow = 2 * cs + (wave >> 2);
  const unsigned cq = (lane >> 2) & 3, cpiece = (lane >> 4) * 4 + (lane & 3);
  unsigned ctag = __builtin_amdgcn_readlane(ep, crow & 31);
  unsigned rbase = WS_REC + crow * MAXS * REC;
  u32x32 A;
  u32x16 P;
  auto contributors = [&](int k) {
#pragma unroll
    for (int g = 0; g < NG; ++g) {
      A[g] = *reinterpret_cast<const unsigned*>(smem + L_CL + 4 * (k + 4 * g + cq)) + ((wave & 3) * 256 + cpiece * 16);
      A[6 + g] = (lane & 1) ? 0u : __float_as_uint(LOWEST);
      A[12 + 2 * g] = 0u; A[13 + 2 * g] = 0u;
      const u64 in = __builtin_amdgcn_ballot_w64((int)(k + 4 * g + cq) < ncon);
      P[2 * g] = (unsigned)in; P[2 * g + 1] = (unsigned)(in >> 32);
    }
    P[12] = min(ncon - k + 3, 4 * NG) >> 2; P[13] = 1u << 17; P[14] = ctag; P[15] = ctag ^ (ctag << 1);
  };
  contributors(0);
  unsigned outa = (((crow >> 2) * NH + head * 4 + (crow & 3)) * DH + 4 * (16 * (wave & 1) + cpiece) + (wave & 2)) * 2;
  const int r0 = 2 * wave + ((actm & 1) ? 0 : 16), r1 = 2 * wave + 16;
  const unsigned t00 = __builtin_amdgcn_readlane(ep, r0), t01 = __builtin_amdgcn_readlane(ep, r0 + 1);
  const unsigned t10 = __builtin_amdgcn_readlane(ep, r1), t11 = __builtin_amdgcn_readlane(ep, r1 + 1);
  unsigned tg0 = hh ? t01 : t00, tg1 = hh ? t11 : t10, th0 = tg0 << 1, th1 = tg1 << 1;
  const int rm = ((lane & 8) ? r1 : r0) + hh;
  unsigned mla = (unsigned)(size_t)smem + L_ML + (lane & 7) * 264 + rm * 8, mlw = (unsigned)(size_t)smem + L_ML + wave * 264 + col * 8;
  unsigned oa = (unsigned)(size_t)smem + (lane & 16) * 512 + (r0 + hh) * 256 + 16 * ((lane & 15) ^ ((r0 + hh) & 15)), oa2 = oa + 65536;
  unsigned sa = (r0 + hh) * (MAXS * REC) + col * 16;
  unsigned so = WS_REC + slot * REC, so1 = so + 16 * MAXS * REC;
  asm volatile("" : "+{v[224:255]}"(A), "+v"(outa));
  asm volatile("" : "+v"(tg0), "+v"(tg1), "+v"(th0), "+v"(th1), "+v"(mla), "+v"(mlw), "+v"(oa), "+v"(oa2), "+v"(sa));
  u32x4 srdW = {(unsigned)wsb, (unsigned)(wsb >> 32) & 0xffffu, WS_HEAD, 0x00020000u};
  asm volatile("" : "+s"(so), "+s"(so1), "+s"(ctag), "+s"(rbase), "+s"(srdW), "+{s[68:83]}"(P));
  if (TOUCH) asm volatile("buffer_load_dword v255, off, %1, 0 offset:512 sc1" : "+{v[224:255]}"(A) : "s"(srdW) : "memory");

  {
    if (TOUCH) { if (tot & 1) WAITVM(5); else WAITVM(9); } else { if (tot & 1) WAITVM(4); else WAITVM(8); }
    bf16x8 kf[8];
    readk(kf);
    __builtin_amdgcn_sched_barrier(0);
    scores(kf);
    *(__attribute__((address_space(3))) f32x2*)mlw = f32x2{ms, swap32_sum(lsum)};
    if (TOUCH) WAITVM(1); else WAITVM(0);
    bf16x8 vf[4][2];
    readv(vf);
    __builtin_amdgcn_sched_barrier(0);
#pragma unroll
    for (int c = 0; c < 4; ++c) {
      acc[c] = __builtin_amdgcn_mfma_f32_32x32x16_bf16(vf[c][0], pf[0], acc[c], 0, 0, 0);
      acc[c] = __builtin_amdgcn_mfma_f32_32x32x16_bf16(vf[c][1], pf[1], acc[c], 0, 0, 0);
      __builtin_amdgcn_sched_barrier(0);
    }
    STAMP(4);
#pragma unroll
    for (int c = 0; c < 4; ++c)
#pragma unroll
      for (int g = 0; g < 4; ++g)
        *(__attribute__((address_space(3))) f32x4*)(ka8[4 * (c & 1) + g] + (c >> 1) * 8192) = f32x4{acc[c][4 * g], acc[c][4 * g + 1], acc[c][4 * g + 2], acc[c][4 * g + 3]};
  }
  asm volatile("s_waitcnt lgkmcnt(0)\n\ts_barrier" ::: "memory");
  auto merge = [&](auto NP) {
    constexpr int np = decltype(NP)::value;
    const f32x2 ml = *(const __attribute__((address_space(3))) f32x2*)mla;
    __builtin_amdgcn_sched_barrier(0);
    f32x4 o[np][NW];
#pragma unroll
    for (int p = 0; p < np; ++p)
#pragma unroll
      for (int w = 0; w < NW; ++w) {
        o[p][w] = *(const __attribute__((address_space(3))) f32x4*)((w < 4 ? oa : oa2) + p * 4096 + (w & 3) * 16384);
        __builtin_amdgcn_sched_barrier(0);
      }
    float M, f;
    asm volatile(
        "v_max_f32_dpp %0, %2, %2 quad_perm:[1,0,3,2] row_mask:0xf bank_mask:0xf\n\t"
        "s_nop 1\n\t"
        "v_max_f32_dpp %0, %0, %0 quad_perm:[2,3,0,1] row_mask:0xf bank_mask:0xf\n\t"
        "s_nop 1\n\t"
        "v_max_f32_dpp %0, %0, %0 row_half_mirror row_mask:0xf bank_mask:0xf\n\t"
        "v_sub_f32 %1, %2, %0\n\t"
        "v_exp_f32 %1, %1\n\t"
        "s_nop 1"
        : "=&v"(M), "=&v"(f) : "v"(ml[0]));
    float L = ml[1] * f;
    L += dppf<0xb1>(L); L += dppf<0x4e>(L); L += dppf<0x141>(L);
    const int st = __float_as_int(__builtin_amdgcn_inverse_ballot_w64(0xaaaaaaaaaaaaaaaaull) ? L : M);
    {
      f32x4 a = o[0][0] * dppf<0x150>(f);
      a += o[0][1] * dppf<0x151>(f); a += o[0][2] * dppf<0x152>(f); a += o[0][3] * dppf<0x153>(f);
      a += o[0][4] * dppf<0x154>(f); a += o[0][5] * dppf<0x155>(f); a += o[0][6] * dppf<0x156>(f); a += o[0][7] * dppf<0x157>(f);
      const unsigned s0 = __builtin_amdgcn_update_dpp(st, st, 0x118, 0xf, 0xf, false), w0 = s0 ^ tg0, w1 = s0 ^ th0;
      put2(u32x4{__float_as_uint(a[0]), w0, __float_as_uint(a[1]), w1}, u32x4{__float_as_uint(a[2]), w0, __float_as_uint(a[3]), w1}, sa, srdW, so);
    }
    if constexpr (np == 2) {
      f32x4 a = o[1][0] * dppf<0x158>(f);
      a += o[1][1] * dppf<0x159>(f); a += o[1][2] * dppf<0x15a>(f); a += o[1][3] * dppf<0x15b>(f);
      a += o[1][4] * dppf<0x15c>(f); a += o[1][5] * dppf<0x15d>(f); a += o[1][6] * dppf<0x15e>(f); a += o[1][7] * dppf<0x15f>(f);
      const unsigned s1 = __builtin_amdgcn_update_dpp(st, st, 0x108, 0xf, 0xf, false), w0 = s1 ^ tg1, w1 = s1 ^ th1;
      put2(u32x4{__float_as_uint(a[0]), w0, __float_as_uint(a[1]), w1}, u32x4{__float_as_uint(a[2]), w0, __float_as_uint(a[3]), w1}, sa, srdW, so1);
    }
  };
  if (actm == 3) merge(IC<2>{}); else if (actm != 0) merge(IC<1>{});
  STAMP(5);

  if (cs >= 0) {
    asm volatile("s_barrier" ::: "memory");
    float M = LOWEST, L = 0.f;
    f32x2 a2 = {0.f, 0.f};
    u32x32 B, C;
    u32x8 D;
    asm volatile("" : "={v[128:159]}"(B), "={v[160:191]}"(C), "={v[192:199]}"(D));
    for (int k = 4 * NG;; k += 4 * NG) {
#define LOADG(b, g) \
          "s_mov_b64 exec, s[68+2*" #g ":69+2*" #g "]\n\t" \
          "s_cbranch_execz 1f\n\t" \
          "buffer_load_dwordx4 v[" #b "+4*" #g ":" #b "+4*" #g "+3], v[224+" #g "], %5, %6 offen sc1\n\t" \
          "1:\n\t"
#define STEPG(b, g) \
          "s_mov_b64 exec, s[68+2*" #g ":69+2*" #g "]\n\t" \
          "s_cbranch_execz 1f\n\t" \
          "v_xor_b32_dpp v254, v[" #b "+4*" #g "+1], v[" #b "+4*" #g "+3] quad_perm:[2,3,0,1] row_mask:0xf bank_mask:0xf\n\t" \
          "v_cmp_ne_u32 vcc, s83, v254\n\t" \
          "s_wqm_b64 s[68+2*" #g ":69+2*" #g "], vcc\n\t" \
          "s_addc_u32 s80, s80, -1\n\t" \
          "s_andn2_b64 exec, exec, s[68+2*" #g ":69+2*" #g "]\n\t" \
          "v_xor_b32 v[230+" #g "], s82, v[" #b "+4*" #g "+1]\n\t" \
          "v_pk_mov_b32 v[236+2*" #g ":237+2*" #g "], v[" #b "+4*" #g ":" #b "+4*" #g "+1], v[" #b "+4*" #g "+2:" #b "+4*" #g "+3] op_sel:[0,0]\n\t" \
          LOADG(b, g)
#define LOADS(b) LOADG(b, 0) LOADG(b, 1) LOADG(b, 2) LOADG(b, 3) LOADG(b, 4) LOADG(b, 5)
#define ROUND(b) \
          "s_add_u32 s88, s88, s89\n\t" \
          "s_lshl3_add_u32 s86, s88, s84\n\t" \
          "s_addc_u32 s87, s85, 0\n\t" \
          "s_mov_b32 s88, s89\n\t" \
          "s_swappc_b64 s[90:91], s[86:87]\n\t" \
          STEPG(b, 0) STEPG(b, 1) STEPG(b, 2) STEPG(b, 3) STEPG(b, 4) STEPG(b, 5) \
          "s_cmp_eq_u32 s80, 0\n\t" \
          "s_cbranch_scc1 7f\n\t" \
          "s_mov_b32 s89, s80\n\t" \
          "s_cmp_lt_u32 s80, %9\n\t" \
          "s_cbranch_scc1 1f\n\t" \
          "s_sleep %8\n\t" \
          "1:\n\t"
#define WENT(n) "s_waitcnt vmcnt(" #n ")\n\ts_setpc_b64 s[90:91]\n\t"
      asm volatile(
          LOADS(128)
          "s_getpc_b64 s[84:85]\n\t"
          "9:\n\t"
          "s_add_u32 s84, s84, 8f-9b\n\t"
          "s_addc_u32 s85, s85, 0\n\t"
          "s_mov_b32 s88, s80\n\t"
          "s_mov_b32 s89, s80\n\t"
          "s_sleep %7\n\t"
          LOADS(152)
          "s_sleep %7\n\t"
          LOADS(176)
          "6:\n\t"
          ROUND(128) ROUND(152) ROUND(176)
          "s_sub_u32 s81, s81, 1\n\t"
          "s_cbranch_scc0 6b\n\t"
          "s_branch 7f\n\t"
          "8:\n\t"
          WENT(0) WENT(1) WENT(2) WENT(3) WENT(4) WENT(5) WENT(6) WENT(7) WENT(8) WENT(9) WENT(10) WENT(11) WENT(12)
          "7:\n\t"
          "s_mov_b64 exec, -1"
          : "+{v[224:255]}"(A), "+{v[128:159]}"(B), "+{v[160:191]}"(C), "+{v[192:199]}"(D), "+{s[68:83]}"(P)
          : "s"(srdW), "s"(rbase), "n"(GAP), "n"(PACE), "n"(PACE_T)
          : "vcc", "scc", "memory", "s84", "s85", "s86", "s87", "s88", "s89", "s90", "s91");
      float Mn = M;
#pragma unroll
      for (int g = 0; g < NG; ++g) Mn = fmaxf(Mn, __uint_as_float(A[6 + g]));
      asm volatile(
          "s_nop 1\n\t"
          "v_max_f32_dpp %0, %0, %0 row_ror:8 row_mask:0xf bank_mask:0xf\n\t"
          "s_nop 1\n\t"
          "v_max_f32_dpp %0, %0, %0 row_ror:4 row_mask:0xf bank_mask:0xf"
          : "+v"(Mn));
      if (k > 4 * NG) {
        const float al = dppf<0xa0>(__builtin_amdgcn_exp2f(M - Mn));
        L *= al; a2 *= al;
      }
      M = Mn;
#pragma unroll
      for (int g = 0; g < NG; ++g) {
        const float f = dppf<0xa0>(__builtin_amdgcn_exp2f(__uint_as_float(A[6 + g]) - Mn));
        L = fmaf(__uint_as_float(A[6 + g]), f, L);
        a2 += f32x2{__uint_as_float(A[12 + 2 * g]), __uint_as_float(A[13 + 2 * g])} * f;
      }
      if (k >= ncon) break;
      WAITVM(0);
      contributors(k);
    }
    STAMP(3);
    float a0 = a2[0], a1 = a2[1];
    L += dppf<0x128>(L); a0 += dppf<0x128>(a0); a1 += dppf<0x128>(a1);
    L += dppf<0x124>(L); a0 += dppf<0x124>(a0); a1 += dppf<0x124>(a1);
    const float inv = dppf<0xf5>(__builtin_amdgcn_rcpf(L));
    const bf16x2 r = __builtin_convertvector((f32x2{a0 * inv, a1 * inv}), bf16x2);
    if (__builtin_amdgcn_inverse_ballot_w64(0x000f000f000f000full)) {
      *reinterpret_cast<__attribute__((address_space(1))) bf16x2*>(ob + (u64)outa) = r;
      if (lane == 0 && (wave & 3) == 0)
        __builtin_amdgcn_raw_buffer_store_b32(ctag, __builtin_amdgcn_make_buffer_rsrc((void*)wsb, 0, WS_HEAD, 0x00020000), crow * 4, 0, 16);
    }
    asm volatile("" :: "{v[128:159]}"(B), "{v[160:191]}"(C), "{v[192:199]}"(D) : "memory");
  }
}

extern "C" void kernel_launch(void* const* d_in, const int* in_sizes, int n_in, void* d_out, int out_size, void* d_ws, size_t ws_size,
                              hipStream_t stream) {
  const unsigned kv_bytes = (unsigned)in_sizes[1] * 2u;
  attn_main<<<256, NTHR, 0, stream>>>(d_in[0], d_in[1], d_in[2], d_in[3], d_in[4], d_ws, d_out, kv_bytes, 0u);
}
```

```cpp
#include <hip/hip_runtime.h>
#include <hip/hip_bf16.h>
#include <stdint.h>

#define NW 8
#define PRIO 0
#define PACE 1
#define GAP 8
#define FEW 3
#define PBIAS 0
#define DEAL 1
#define GATE 8
#define TC_A 36
#define TC_B 26
#define TC_C 33
#define LO_A 4
#define LO_B 3
#define LO_C 4
#define PD_A 1
#define PD_B 1
#define PD_C 1
#define FD_A 1
#define FD_B 1
#define FD_C 1
#define WARM 0
#define GPOS 1
#define CP 3
#define CP2 3
#define STR_(x) #x
#define STR(x) STR_(x)
#define EV 2
#define EV1 (EV == 1 ? 8 : EV == 3 || EV == 4 ? 4 : 0)
#define ILV 2
#define NS_A 29
#define NS_B 39
#define NS_C 33

typedef __attribute__((ext_vector_type(8))) __bf16 bf16x8;
typedef __attribute__((ext_vector_type(4))) __bf16 bf16x4;
typedef __attribute__((ext_vector_type(2))) __bf16 bf16x2;
typedef __attribute__((ext_vector_type(2))) float f32x2;
typedef __attribute__((ext_vector_type(4))) float f32x4;
typedef __attribute__((ext_vector_type(16))) float f32x16;
typedef __attribute__((ext_vector_type(2))) unsigned u32x2;
typedef __attribute__((ext_vector_type(4))) unsigned u32x4;
typedef __attribute__((ext_vector_type(8))) int i32x8;
typedef __attribute__((ext_vector_type(8))) float f32x8;
typedef __attribute__((ext_vector_type(32))) unsigned u32x32;
typedef __attribute__((ext_vector_type(16))) unsigned u32x16;
typedef __attribute__((address_space(3))) bf16x4* lds_bf16x4_p;
typedef unsigned long long u64;
template <int N> struct IC { static constexpr int value = N; };

constexpr int NBATCH = 8, NH = 32, NHKV = 8, DH = 128, PAGE = 16, NBLK = 512;
constexpr int NTHR = NW * 64;
static_assert(5 * NS_A + 2 * NS_B + NS_C == 256, "grid");
constexpr int MAXS = 40;
constexpr int MAXU = 208;
constexpr int NP_A = NS_A - NBATCH, NP_B = NS_B - NBATCH, NP_C = NS_C - NBATCH;
static_assert(NP_A >= 8 && NS_B >= NS_A && NS_C >= NS_A && NP_B <= 32 && NP_C <= 32 && 4096 / NP_A + 2 + 4 <= MAXU && PD_A <= 4 && PD_B <= 4 && PD_C <= 4, "limits");
static_assert(TC_A >= 8 * LO_A && TC_B >= 8 * LO_B && TC_C >= 8 * LO_C && LO_A >= 1 && LO_B >= 1 && LO_C >= 1, "limits");
constexpr int PAGE_BYTES = PAGE * NHKV * DH * 2;
constexpr int TOK_BYTES = NHKV * DH * 2;
constexpr float SCALE_LOG2E = 0.08838834764831845f * 1.4426950408889634f;
constexpr float LOWEST = -3.0e38f;

constexpr int L_TAB = NW * 16384;
constexpr int L_Q = L_TAB + NBLK * NBATCH * 4;
constexpr int L_CNT = L_Q + 32 * DH * 2;
constexpr int L_CM = L_CNT + 64;
constexpr int L_GC = L_CM + 64;
constexpr int L_LIST = L_GC + 16;
constexpr int L_ML = L_LIST + MAXU * 4;
constexpr int L_JUNK = L_ML + NW * 264;
constexpr int LDS_BYTES = L_JUNK + 256;
static_assert(ILV == 2 || GPOS != 2, "gate");
static_assert(LDS_BYTES <= 163840 && (L_LIST & 15) == 0, "lds");

constexpr unsigned WS_HEAD = 1u << 21;
constexpr unsigned WS_REC = 1024;
constexpr unsigned REC = 1024;
static_assert(WS_REC + 32u * MAXS * REC <= WS_HEAD, "ws");

__device__ __forceinline__ float swap32_max(float x) {
  auto r = __builtin_amdgcn_permlane32_swap(__float_as_uint(x), __float_as_uint(x), false, false);
  return fmaxf(__uint_as_float(r[0]), __uint_as_float(r[1]));
}
__device__ __forceinline__ float swap32_sum(float x) {
  auto r = __builtin_amdgcn_permlane32_swap(__float_as_uint(x), __float_as_uint(x), false, false);
  return __uint_as_float(r[0]) + __uint_as_float(r[1]);
}
__device__ __forceinline__ float swap16_max(float x) {
  auto r = __builtin_amdgcn_permlane16_swap(__float_as_uint(x), __float_as_uint(x), false, false);
  return fmaxf(__uint_as_float(r[0]), __uint_as_float(r[1]));
}
__device__ __forceinline__ float swap16_sum(float x) {
  auto r = __builtin_amdgcn_permlane16_swap(__float_as_uint(x), __float_as_uint(x), false, false);
  return __uint_as_float(r[0]) + __uint_as_float(r[1]);
}
__device__ __forceinline__ unsigned next_tag(unsigned e) { return e * 1664525u + 1013904223u; }
__device__ __forceinline__ int mbcnt64(u64 m, int base) {
  return (int)__builtin_amdgcn_mbcnt_hi((unsigned)(m >> 32), __builtin_amdgcn_mbcnt_lo((unsigned)m, (unsigned)base));
}
template <int CTRL, int RM> __device__ __forceinline__ int dpp0(int x) { return __builtin_amdgcn_update_dpp(0, x, CTRL, RM, 0xf, false); }
template <int CTRL> __device__ __forceinline__ float dppf(float x) {
  return __int_as_float(__builtin_amdgcn_mov_dpp(__float_as_int(x), CTRL, 0xf, 0xf, true));
}

__device__ __forceinline__ void put2(u32x4 a, u32x4 b, unsigned voff, u32x4 srd, unsigned soff) {
  asm volatile("buffer_store_dwordx4 %0, %2, %3, %4 offen sc1\n\tbuffer_store_dwordx4 %1, %2, %3, %4 offen offset:512 sc1\n\ts_nop 1"
               :: "v"(a), "v"(b), "v"(voff), "s"(srd), "s"(soff) : "memory");
}
__device__ __forceinline__ void dma_page(unsigned ldsaddr, u32x4 srd, unsigned soff, unsigned v0, unsigned v1, unsigned v2, unsigned v3) {
  asm volatile(
      "s_nop 4\n\t"
      "s_mov_b32 m0, %0\n\t"
      "s_nop 0\n\t"
      "buffer_load_dwordx4 %3, %1, %2 offen nt lds\n\t"
      "buffer_load_dwordx4 %4, %1, %2 offen offset:1024 nt lds\n\t"
      "buffer_load_dwordx4 %5, %1, %2 offen offset:2048 nt lds\n\t"
      "buffer_load_dwordx4 %6, %1, %2 offen offset:3072 nt lds"
      :: "s"(ldsaddr), "s"(srd), "s"(soff), "v"(v0), "v"(v1), "v"(v2), "v"(v3) : "memory");
}
#define WAITVM(n) asm volatile("s_waitcnt vmcnt(" #n ")" ::: "memory")
__device__ __forceinline__ void wait_pages(int np) {
  if (np >= 2) WAITVM(8); else if (np == 1) WAITVM(4); else WAITVM(0);
}

#define CLOBBER \
  "s48", "s49", "s50", "s51", "s52", "s53", "s54", "s55", "s56", "s57", "s58", "s59", "s60", "s61", "s62", "s63", "s64", "s65", "s66", "s67", \
  "s68", "s69", "s70", "s71", "s72", "s73", "s74", "s75", "s76", "s77", "s78", "s79", "s80", "s81", "s82", "s83", "s84", "s85", "s86", "s87", \
  "s88", "s89", "s90", "s91", "s92", "s93", "s94", "s95", "v64", "v65", "v66", "v67", "v68", "v69", "v70", "v71", "v72", "v73", "v74", "v75", \
  "v76", "v77", "v78", "v79", "v80", "v81", "v82", "v83", "v84", "v85", "v86", "v87", "v88", "v89", "v90", "v91", "v92", "v93", "v94", "v95", \
  "v96", "v97", "v98", "v99", "v100", "v101", "v102", "v103", "v104", "v105", "v106", "v107", "v108", "v109", "v110", "v111", "v112", "v113", "v114", "v115", \
  "v116", "v117", "v118", "v119", "v120", "v121", "v122", "v123", "v124", "v125", "v126", "v127", "v128", "v129", "v130", "v131", "v132", "v133", "v134", "v135", \
  "v136", "v137", "v138", "v139", "v140", "v141", "v142", "v143", "v144", "v145", "v146", "v147", "v148", "v149", "v150", "v151", "v152", "v153", "v154", "v155", \
  "v156", "v157", "v158", "v159", "v160", "v161", "v162", "v163", "v164", "v165", "v166", "v167", "v168", "v169", "v170", "v171", "v172", "v173", "v174", "v175", \
  "v176", "v177", "v178", "v179", "v180", "v181", "v182", "v183", "v184", "v185", "v186", "v187", "v188", "v189", "v190", "v191", "v224", "v225", "v226", "v227", \
  "v228", "v229", "v230", "v231", "v232", "v233", "v234", "v235", "v240", "v241", "v242"
#define STAMP(k) do {} while (0)

__global__ void __launch_bounds__(NTHR, 2)
attn_main(const void* q_, const void* kc_, const void* vc_, const void* bt_, const void* ctx_, void* ws_, void* out_, unsigned kv_bytes_, unsigned pad_) {
  __shared__ __attribute__((aligned(16))) char smem[LDS_BYTES];
  const int tid = threadIdx.x, lane = tid & 63;
  const int wave = __builtin_amdgcn_readfirstlane(tid >> 6);
  int bid = blockIdx.x;
  const int col = lane & 31, hh = lane >> 5;
  STAMP(0);
  u32x16 ka;
  asm volatile("s_load_dwordx16 %0, %2, 0x0\n\ts_setprio 3" : "=&s"(ka), "+s"(bid) : "s"(__builtin_amdgcn_kernarg_segment_ptr()));

  constexpr int HALF = 3 * NS_A + NS_B;
  const bool up = bid >= HALF;
  const int rel = up ? bid - HALF : bid;
  const bool c1 = rel >= NS_A, c2 = rel >= NS_A + NS_B, c3 = rel >= 2 * NS_A + NS_B;
  int cls = (up ? 4 : 0) + (c3 ? 3 : c2 ? 2 : c1 ? 1 : 0);
  int slot = rel - (c3 ? 2 * NS_A + NS_B : c2 ? NS_A + NS_B : c1 ? NS_A : 0);
  int np = cls == 7 ? NP_C : (cls & 3) == 1 ? NP_B : NP_A;
  int tc = cls == 7 ? TC_C : (cls & 3) == 1 ? TC_B : TC_A;
  int lor = cls == 7 ? LO_C : (cls & 3) == 1 ? LO_B : LO_A;
  unsigned magic = cls == 7 ? 0xffffffffu / NP_C + 1u : (cls & 3) == 1 ? 0xffffffffu / NP_B + 1u : 0xffffffffu / NP_A + 1u;
  int be = (int)__umulhi((unsigned)(slot * NW + wave), magic), jp = slot * NW + wave - be * np;
  asm volatile("" : "+s"(be), "+s"(jp));
  be = slot >= np ? slot - np : be;
  int pd = cls == 7 ? PD_C : (cls & 3) == 1 ? PD_B : PD_A, par0 = bid - slot;
  int fd = cls == 7 ? FD_C : (cls & 3) == 1 ? FD_B : FD_A;
  asm volatile("" : "+s"(pd), "+s"(fd));
  int keepe = 2 * (np + tc - (((par0 + np) & 1) ? fd : 0)) * PAGE, keepo = 2 * (np + tc - (((par0 + np) & 1) ? 0 : fd)) * PAGE;
  tc -= ((par0 + np + be) & 1) ? fd : 0;
  jp = 2 * (tc + jp);
  int dlo = ((bid & 1) && slot > 0) ? pd : 0, dhi = (!(bid & 1) && slot + 1 < np) ? pd : 0;
  asm volatile("" : "+s"(keepe), "+s"(keepo), "+s"(jp), "+s"(dlo), "+s"(dhi), "+s"(pd), "+s"(par0));
  unsigned ring = (unsigned)(size_t)smem + wave * 16384;
  unsigned rowa = be * (NBLK * 4) + lane * 16, taa = tid * 4 + 4096, tab2 = tid * 4 + 12288;
  unsigned be4 = be * 4, rowl = ring + 8192;
  asm volatile("" : "+v"(rowa), "+v"(taa), "+v"(tab2));
  asm volatile("s_waitcnt lgkmcnt(0)" : "+s"(ka), "+s"(cls), "+s"(slot), "+s"(np), "+s"(tc), "+s"(lor), "+s"(magic), "+s"(be), "+s"(ring), "+s"(be4), "+s"(rowl));
  const u64 qb = ((u64)ka[1] << 32) | ka[0], btb = ((u64)ka[7] << 32) | ka[6], cb = ((u64)ka[9] << 32) | ka[8];
  const u64 ws0 = ((u64)ka[11] << 32) | ka[10], ob = ((u64)ka[13] << 32) | ka[12];

  int id[NBATCH];
  i32x8 cl;
  int cle;
  asm volatile(
      "s_mov_b32 m0, %16\n\t"
      "s_load_dword %9, %13, %14\n\t"
      "global_load_lds_dwordx4 %15, %12\n\t"
      "global_load_lds_dwordx4 %15, %12 offset:1024\n\t"
      "s_load_dwordx8 %8, %13, 0x0\n\t"
      "global_load_dword %0, %10, %12 offset:-4096\n\t"
      "global_load_dword %1, %10, %12 offset:-2048\n\t"
      "global_load_dword %2, %10, %12\n\t"
      "global_load_dword %3, %10, %12 offset:2048\n\t"
      "global_load_dword %4, %11, %12 offset:-4096\n\t"
      "global_load_dword %5, %11, %12 offset:-2048\n\t"
      "global_load_dword %6, %11, %12\n\t"
      "global_load_dword %7, %11, %12 offset:2048"
      : "=&v"(id[0]), "=&v"(id[1]), "=&v"(id[2]), "=&v"(id[3]), "=&v"(id[4]), "=&v"(id[5]), "=&v"(id[6]), "=&v"(id[7]), "=&s"(cl), "=&s"(cle)
      : "v"(taa), "v"(tab2), "s"(btb), "s"(cb), "s"(be4), "v"(rowa), "s"(rowl) : "memory");

  int head = (cls - (int)(ka[2] >> 8)) & 7;
  const u64 wsb = ws0 + (u64)(head * WS_HEAD);
  unsigned ep; int myc;
  asm volatile(
      "s_mov_b32 m0, %8\n\t"
      "s_nop 0\n\t"
      "global_load_lds_dwordx4 %2, %3\n\t"
      "global_load_dword %1, %6, %7"
      : "=&v"(ep), "=&v"(myc)
      : "v"((lane & 48) * 16 + 16 * ((lane ^ (4 * wave + (lane >> 4))) & 15)), "s"(qb + (u64)((wave * NH + head * 4) * DH * 2)), "v"(col * 4),
        "s"(wsb), "v"(col & 28), "s"(cb), "s"((unsigned)(size_t)smem + L_Q + wave * 1024) : "memory");
  if (WARM) {
    const unsigned ix = (bid & 0x78) + wave;
    const u64 wb = (bid >= 128 ? ((u64)ka[5] << 32) | ka[4] : ((u64)ka[3] << 32) | ka[2]) + (u64)(((ka[14] >> 7) * ix) & ~127u);
    asm volatile("s_mov_b32 m0, %2\n\ts_mov_b64 exec, 1\n\tglobal_load_lds_dword %0, %1\n\ts_mov_b64 exec, -1" :: "v"(0), "s"(wb), "s"((unsigned)(size_t)smem + L_JUNK) : "memory");
  }
  const unsigned g4 = lane >> 4, p16 = lane & 15;
  unsigned vk[4], vv[4];
#pragma unroll
  for (int i = 0; i < 4; ++i) {
    vk[i] = (4 * i + g4) * TOK_BYTES + 16 * (p16 ^ (4 * i + g4)) - 1024 * i;
    vv[i] = (4 * i + g4) * TOK_BYTES + 16 * (p16 ^ (g4 << 2)) - 1024 * i;
  }
  const bool fin = slot >= np;
  int je2;
  {
    const int rd = (lane + 1) & 63;
    int t0 = NW * rd + wave, t1 = 4 * rd + (wave >= 4 ? 4 * (lor - 1) + wave : NBLK);
    asm volatile("" : "+v"(t0), "+v"(t1));
    const int t = DEAL ? NW * rd + (wave ^ 4) : rd < lor ? t0 : t1;
    je2 = t < (fin ? tc : 0) ? 2 * t : (fin ? 2 * NBLK : jp);
  }
  asm volatile("" : "+v"(vk[0]), "+v"(vk[1]), "+v"(vk[2]), "+v"(vk[3]), "+v"(vv[0]), "+v"(vv[1]), "+v"(vv[2]), "+v"(vv[3]), "+v"(je2));

  u32x4 srdK = {ka[2], ka[3] & 0xffffu, ka[14], 0x00020000u};
  u32x4 srdV = {ka[4], ka[5] & 0xffffu, ka[14], 0x00020000u};
  unsigned hoff = head * (DH * 2);
  asm volatile("s_waitcnt lgkmcnt(0)" : "+s"(cl), "+s"(cle), "+s"(srdK), "+s"(srdV), "+s"(hoff) :: "memory");
  const int colBv = (int)(min((unsigned)cle - 1u, (unsigned)(NBLK * PAGE - 1)) >> 4) - je2;
  const int cAv = max(colBv - 1, 0);
  u32x2 e2;
  asm volatile("s_waitcnt vmcnt(%2)\n\tds_read2_b32 %0, %1 offset1:1\n\ts_waitcnt lgkmcnt(0)" : "=v"(e2) : "v"(rowl + 4 * cAv), "n"(10 + WARM) : "memory");
  const unsigned pa0 = (unsigned)__builtin_amdgcn_readlane(e2[0], 63) * PAGE_BYTES + hoff;
  const unsigned pb0 = (unsigned)__builtin_amdgcn_readlane(e2[1], 63) * PAGE_BYTES + hoff;
  dma_page(ring, srdK, pa0, vk[0], vk[1], vk[2], vk[3]);
  dma_page(ring + 4096, srdK, pb0, vk[0], vk[1], vk[2], vk[3]);
  if (EV1 >= 4) dma_page(ring + 8192, srdV, pa0, vv[0], vv[1], vv[2], vv[3]);
  if (EV1 >= 8) dma_page(ring + 12288, srdV, pb0, vv[0], vv[1], vv[2], vv[3]);
  __builtin_amdgcn_s_setprio(0);

  u64 live[NBATCH], lead[NBATCH];
  {
    const unsigned t16[2] = {(unsigned)(tid * PAGE + keepe), (unsigned)(tid * PAGE + keepo)};
#pragma unroll
    for (int b = 0; b < NBATCH; ++b) live[b] = __builtin_amdgcn_ballot_w64(t16[b & 1] <= min((unsigned)cl[b] - 1u, (unsigned)(NBLK * PAGE - 1)));
  }
  asm volatile("s_waitcnt vmcnt(%16)"
               : "+v"(id[0]), "+v"(id[1]), "+v"(id[2]), "+v"(id[3]), "+v"(id[4]), "+v"(id[5]), "+v"(id[6]), "+v"(id[7]),
                 "+s"(live[0]), "+s"(live[1]), "+s"(live[2]), "+s"(live[3]), "+s"(live[4]), "+s"(live[5]), "+s"(live[6]), "+s"(live[7]) : "n"(10 + WARM + EV1) : "memory");
#pragma unroll
  for (int b = 0; b < NBATCH; ++b) id[b] = __builtin_amdgcn_inverse_ballot_w64(live[b]) ? id[b] : -1 - b;
  *reinterpret_cast<int4*>(smem + L_TAB + tid * 32) = make_int4(id[0], id[1], id[2], id[3]);
  *reinterpret_cast<int4*>(smem + L_TAB + tid * 32 + 16) = make_int4(id[4], id[5], id[6], id[7]);
  {
    unsigned pk[2] = {0u, 0u};
#pragma unroll
    for (int b = 0; b < NBATCH; ++b) {
      u64 dup = 0;
#pragma unroll
      for (int e = 0; e < b; ++e) dup |= __builtin_amdgcn_ballot_w64(id[e] == id[b]);
      lead[b] = live[b] & ~dup;
      pk[b >> 2] |= (unsigned)__builtin_popcountll(lead[b]) << (8 * (b & 3));
    }
    if (lane == 0) { *reinterpret_cast<uint2*>(smem + L_CNT + 8 * wave) = make_uint2(pk[0], pk[1]); *reinterpret_cast<uint2*>(smem + L_GC) = make_uint2(0u, 0u); }
  }
  asm volatile("s_waitcnt vmcnt(%2) lgkmcnt(0)\n\ts_barrier" : "+v"(myc) : "n"(0), "n"(8 + WARM + EV1) : "memory");
  static_assert(EV == 2, "order");
  asm volatile("global_load_dword %0, %1, %2 sc1" : "=&v"(ep) : "v"(col * 4), "s"(wsb) : "memory");
  if (EV == 2 || EV == 5) dma_page(ring + 8192, srdV, pa0, vv[0], vv[1], vv[2], vv[3]);
  if (EV == 2 || EV == 4) dma_page(ring + 12288, srdV, pb0, vv[0], vv[1], vv[2], vv[3]);
  int n;
  {
    const int own = *reinterpret_cast<const unsigned char*>(smem + L_CNT + (lane & 7) * 8 + (lane >> 3));
    int x = own;
    x += dpp0<0x111, 0xf>(x); x += dpp0<0x112, 0xf>(x); x += dpp0<0x114, 0xf>(x); x += dpp0<0x118, 0xf>(x);
    x += dpp0<0x142, 0xa>(x); x += dpp0<0x143, 0xc>(x);
    const int excl = x - own;
    const int U = __builtin_amdgcn_readlane(x, 63);
    const bool many = U >= 8 * np;
    const int lo = (int)__umulhi((unsigned)(slot * U), magic) + (many ? dlo : 0);
    n = fin ? 0 : (int)__umulhi((unsigned)((slot + 1) * U), magic) + (many ? dhi : 0) - lo;
    int idc = id[0];
#pragma unroll
    for (int b = 1; b < NBATCH; ++b) if (be == b) idc = id[b];
    int uc = -1;
#pragma unroll
    for (int b = 0; b < NBATCH; ++b) {
      const int u = mbcnt64(lead[b], __builtin_amdgcn_readlane(excl, b * 8 + wave));
      const bool ld = __builtin_amdgcn_inverse_ballot_w64(lead[b]);
      if (ld && (unsigned)(u - lo) < (unsigned)n) *reinterpret_cast<unsigned*>(smem + L_LIST + 4 * (u - lo)) = ((unsigned)id[b] << 9) | (unsigned)tid;
      if (ld && id[b] == idc) uc = u;
    }
    if (fin) {
      const float ru = __builtin_amdgcn_rcpf((float)U);
      const int s1 = (int)(((float)((uc + 1) * np - 1) + 0.5f) * ru), s2 = (int)(((float)((max(uc - (many ? pd : 0), 0) + 1) * np - 1) + 0.5f) * ru);
      const int s = ((par0 + s1) & 1) ? s2 : s1;
      u64 rest = __builtin_amdgcn_ballot_w64(uc >= 0), cm = 0;
      while (rest) {
        const int s0 = __builtin_amdgcn_readlane(s, __builtin_ctzll(rest));
        cm |= 1ull << s0;
        rest &= ~__builtin_amdgcn_ballot_w64(s == s0);
      }
      if (lane == 0) *reinterpret_cast<u64*>(smem + L_CM + 8 * wave) = cm;
    }
  }
  asm volatile("s_waitcnt lgkmcnt(0)\n\ts_barrier" ::: "memory");
  STAMP(7);
  if (EV == 0) dma_page(ring + 8192, srdV, pa0, vv[0], vv[1], vv[2], vv[3]);
  if (EV == 0 || EV == 3 || EV == 5) dma_page(ring + 12288, srdV, pb0, vv[0], vv[1], vv[2], vv[3]);
  const int ntiles = (n + 1) >> 1;
  int nl = (ntiles > wave) ? (ntiles - wave + NW - 1) / NW : 0;
  int tot = 2 * nl - (((n & 1) && nl > 0 && wave + NW * (nl - 1) == ntiles - 1) ? 1 : 0);
  uint2 ent = *reinterpret_cast<const uint2*>(smem + L_LIST + 8 * (wave + NW * min(lane, MAXU / (2 * NW) - 1)));
  if (fin) {
    nl = __builtin_popcountll(__builtin_amdgcn_ballot_w64(colBv >= 0) << 1);
    tot = 2 * nl - ((__builtin_amdgcn_ballot_w64(colBv == 0) << 1) != 0 ? 1 : 0);
    ent = make_uint2((e2[0] << 9) | (unsigned)cAv, (e2[1] << 9) | (unsigned)(cAv + 1));
  }
  const int colB = __builtin_amdgcn_readlane(colBv, 63), cA = max(colB - 1, 0);

  bool act0, act1;
  {
    const int b0 = wave >> 1, b1 = b0 + 4;
    const int e0 = (int)__umulhi((unsigned)(slot * NW), magic), e1 = (int)__umulhi((unsigned)(slot * NW + NW - 1), magic);
    u64 h0 = 0, h1 = 0;
    for (int r = 0; r < n; r += 64) {
      const unsigned en = *reinterpret_cast<const unsigned*>(smem + L_LIST + 4 * min(r + lane, n - 1));
      const unsigned t0 = *reinterpret_cast<const unsigned*>(smem + L_TAB + (en & 511u) * 32 + 4 * b0);
      const unsigned t1 = *reinterpret_cast<const unsigned*>(smem + L_TAB + (en & 511u) * 32 + 4 * b0 + 16);
      h0 |= __builtin_amdgcn_ballot_w64(t0 == (en >> 9));
      h1 |= __builtin_amdgcn_ballot_w64(t1 == (en >> 9));
    }
    act0 = h0 != 0 || b0 == e0 || b0 == e1;
    act1 = h1 != 0 || b1 == e0 || b1 == e1;
  }
  u64 pend = 0;
  if (fin) {
    const u64 cmv = *reinterpret_cast<const u64*>(smem + L_CM + 8 * (lane & 7));
    unsigned clo = 0, chi = 0;
#pragma unroll
    for (int w = 0; w < NW; ++w) { clo |= __builtin_amdgcn_readlane((unsigned)cmv, w); chi |= __builtin_amdgcn_readlane((unsigned)(cmv >> 32), w); }
    pend = (((u64)chi << 32) | clo) | __builtin_amdgcn_ballot_w64(lane >= ((be * np) >> 3) && lane <= (((be + 1) * np - 1) >> 3));
  }

  bf16x8 qf[8];
#pragma unroll
  for (int s = 0; s < 8; ++s) qf[s] = *reinterpret_cast<const bf16x8*>(smem + L_Q + col * 256 + 16 * ((2 * s + hh) ^ (col & 15)));
  const bool myzero = myc <= 0;
  myc = (myzero ? NBLK * PAGE : min(myc, NBLK * PAGE)) - 4 * hh;
  if (myzero) {
#pragma unroll
    for (int s = 0; s < 8; ++s) qf[s] = bf16x8{};
  }
  int actm = __builtin_amdgcn_readfirstlane((act0 ? 1 : 0) | (act1 ? 2 : 0));
  pend = ((u64)(unsigned)__builtin_amdgcn_readfirstlane((unsigned)(pend >> 32)) << 32) | (unsigned)__builtin_amdgcn_readfirstlane((unsigned)pend);
  pend = __builtin_bitreverse64(pend) >> (64 - np);
  nl = __builtin_amdgcn_readfirstlane(nl); tot = __builtin_amdgcn_readfirstlane(tot);
  u64 finm = __builtin_amdgcn_ballot_w64(fin && (col >> 2) == be), pubm = __builtin_amdgcn_ballot_w64(!fin);
  asm volatile("" : "+s"(actm), "+s"(pend), "+s"(finm), "+s"(pubm), "+s"(nl), "+s"(tot));

  const int pbias = (fin && wave < 4) ? PBIAS : 0;
  auto prio = [&](int left) {
    left += pbias;
    if (PRIO == 2) { if (wave >= 4) __builtin_amdgcn_s_setprio(1); }
    else if (PRIO == 3) { if (left >= 2) __builtin_amdgcn_s_setprio(0); else if (left == 1) __builtin_amdgcn_s_setprio(1); else __builtin_amdgcn_s_setprio(2); }
    else if (PRIO) {
      if (left >= 3) __builtin_amdgcn_s_setprio(3); else if (left == 2) __builtin_amdgcn_s_setprio(2);
      else if (left == 1) __builtin_amdgcn_s_setprio(1); else __builtin_amdgcn_s_setprio(0);
    }
  };
  prio(nl);

  unsigned ka8[8], va4[4];
  {
    const unsigned tq = (lane & 15) >> 2, tp = lane & 3, blk = (lane >> 4) & 1;
#pragma unroll
    for (int s = 0; s < 8; ++s) ka8[s] = ring + col * 256 + 16 * ((2 * s + hh) ^ (col & 15));
#pragma unroll
    for (int c = 0; c < 4; ++c) va4[c] = ring + 8192 + (4 * hh + tq) * 256 + 8 * (tp & 1) + 16 * ((4 * c + 2 * blk + (tp >> 1)) ^ (tq << 2));
    asm volatile("" : "+v"(ka8[0]), "+v"(ka8[1]), "+v"(ka8[2]), "+v"(ka8[3]), "+v"(ka8[4]), "+v"(ka8[5]), "+v"(ka8[6]), "+v"(ka8[7]),
                      "+v"(va4[0]), "+v"(va4[1]), "+v"(va4[2]), "+v"(va4[3]));
  }

  f32x16 acc[4];
#pragma unroll
  for (int c = 0; c < 4; ++c) acc[c] = f32x16{};
  float ms = LOWEST, lsum = 0.f;
  f32x16 sc;
  auto mask = [&](int nva, int nvb) {
#pragma unroll
    for (int r = 0; r < 16; ++r) sc[r] = ((r & 3) + 8 * ((r >> 2) & 1) < (r < 8 ? nva : nvb)) ? 0.f : -INFINITY;
    asm volatile("" : "+v"(sc));
  };
  mask(((col >> 2) == be && colB >= 0) ? myc - cA * PAGE : 0, ((col >> 2) == be && colB >= 1) ? myc - (cA + 1) * PAGE : 0);
  bf16x8 pf[2];

  auto scores = [&](const bf16x8 (&kf)[8]) {
#pragma unroll
    for (int s = 0; s < 8; ++s) sc = __builtin_amdgcn_mfma_f32_32x32x16_bf16(kf[s], qf[s], sc, 0, 0, 0);
    float mx = -INFINITY;
#pragma unroll
    for (int r = 0; r < 16; ++r) mx = fmaxf(mx, sc[r]);
    mx = swap32_max(mx);
    const float mn = fmaxf(ms, mx * SCALE_LOG2E);
    const float alpha = __builtin_amdgcn_exp2f(ms - mn);
    ms = mn;
    float ps = 0.f;
#pragma unroll
    for (int r = 0; r < 16; ++r) { sc[r] = __builtin_amdgcn_exp2f(fmaf(sc[r], SCALE_LOG2E, -mn)); ps += sc[r]; }
    lsum = fmaf(lsum, alpha, ps);
#pragma unroll
    for (int s = 0; s < 2; ++s)
#pragma unroll
      for (int k = 0; k < 4; ++k) {
        const bf16x2 t = __builtin_convertvector((f32x2{sc[8 * s + 2 * k], sc[8 * s + 2 * k + 1]}), bf16x2);
        pf[s][2 * k] = t[0]; pf[s][2 * k + 1] = t[1];
      }
#pragma unroll
    for (int c = 0; c < 4; ++c) acc[c] *= alpha;
    asm volatile("" : "+v"(acc[0]), "+v"(acc[1]), "+v"(acc[2]), "+v"(acc[3]), "+v"(pf[0]), "+v"(pf[1]));
  };
  auto readk = [&](bf16x8 (&kf)[8]) {
#pragma unroll
    for (int s = 0; s < 8; ++s) kf[s] = *(const __attribute__((address_space(3))) bf16x8*)ka8[s];
  };
  auto readv = [&](bf16x8 (&vf)[4][2]) {
#pragma unroll
    for (int c = 0; c < 4; ++c)
#pragma unroll
      for (int s = 0; s < 2; ++s) {
        const bf16x4 x = __builtin_amdgcn_ds_read_tr16_b64_v4bf16((lds_bf16x4_p)(va4[c] + s * 4096));
        const bf16x4 y = __builtin_amdgcn_ds_read_tr16_b64_v4bf16((lds_bf16x4_p)(va4[c] + s * 4096 + 2048));
        vf[c][s] = __builtin_shufflevector(x, y, 0, 1, 2, 3, 4, 5, 6, 7);
      }
  };

#pragma nounroll
  for (int i = 0; i < nl; ++i) {
    const bool two = tot - 2 * i >= 2;
    const unsigned ea = __builtin_amdgcn_readlane(ent.x, i), eb = __builtin_amdgcn_readlane(ent.y, i);
    const unsigned ta = *reinterpret_cast<const unsigned*>(smem + L_TAB + (ea & 511u) * 32 + (col & 28));
    const unsigned tb = *reinterpret_cast<const unsigned*>(smem + L_TAB + (eb & 511u) * 32 + (col & 28));
    const unsigned pa = (ea >> 9) * PAGE_BYTES + hoff, pb = (eb >> 9) * PAGE_BYTES + hoff;
    WAITVM(8);
    bf16x8 kf[8];
    readk(kf);
    asm volatile("s_waitcnt lgkmcnt(0)" ::: "memory");
    prio(nl - 1 - i);
    dma_page(ring, srdK, pa, vk[0], vk[1], vk[2], vk[3]);
    if (two) dma_page(ring + 4096, srdK, pb, vk[0], vk[1], vk[2], vk[3]);
    scores(kf);
    if (two) WAITVM(8); else WAITVM(4);
    bf16x8 vf[4][2];
    readv(vf);
    asm volatile("s_waitcnt lgkmcnt(0)" ::: "memory");
    dma_page(ring + 8192, srdV, pa, vv[0], vv[1], vv[2], vv[3]);
    if (two) dma_page(ring + 12288, srdV, pb, vv[0], vv[1], vv[2], vv[3]);
#pragma unroll
    for (int c = 0; c < 4; ++c) {
      acc[c] = __builtin_amdgcn_mfma_f32_32x32x16_bf16(vf[c][0], pf[0], acc[c], 0, 0, 0);
      acc[c] = __builtin_amdgcn_mfma_f32_32x32x16_bf16(vf[c][1], pf[1], acc[c], 0, 0, 0);
    }
    const u64 ma = (__builtin_amdgcn_ballot_w64(ta == (ea >> 9)) & pubm) | finm, mb = two ? (__builtin_amdgcn_ballot_w64(tb == (eb >> 9)) & pubm) | finm : 0ull;
    mask(__builtin_amdgcn_inverse_ballot_w64(ma) ? myc - (int)(ea & 511u) * PAGE : 0, __builtin_amdgcn_inverse_ballot_w64(mb) ? myc - (int)(eb & 511u) * PAGE : 0);
  }

  auto count = [&](auto OFF) {
    asm volatile("s_mov_b64 exec, 1\n\tds_add_u32 %0, %1 offset:%2\n\ts_mov_b64 exec, -1" :: "v"((unsigned)(size_t)smem + L_GC), "v"(1u), "n"(decltype(OFF)::value) : "memory");
  };
  auto await = [&](int off, int cnt) {
    while (__builtin_amdgcn_readfirstlane(*(const volatile __attribute__((address_space(3))) int*)((unsigned)(size_t)smem + L_GC + off)) < cnt) __builtin_amdgcn_s_sleep(1);
    asm volatile("" ::: "memory");
  };
  if (GPOS == 0) count(IC<0>{});
  if (nl == 0) WAITVM(8);
  asm volatile("" : "+v"(ep) :: "memory");
  ep = next_tag(ep);
  const int crow = 4 * be + (wave & 3);
  unsigned ctag = __builtin_amdgcn_readlane(ep, crow);
  const int r0 = fin ? crow : 2 * wave + ((actm & 1) ? 0 : 16), r1 = 2 * wave + 16;
  const int rh = fin ? 0 : hh;
  const unsigned t00 = __builtin_amdgcn_readlane(ep, r0), t01 = __builtin_amdgcn_readlane(ep, r0 + 1);
  const unsigned t10 = __builtin_amdgcn_readlane(ep, r1), t11 = __builtin_amdgcn_readlane(ep, r1 + 1);
  unsigned tg0 = hh ? t01 : t00, tg1 = hh ? t11 : t10, th0 = tg0 << 1, th1 = tg1 << 1;
  const int rm = (((lane & 8) && !fin) ? r1 : r0) + rh;
  unsigned mla = (unsigned)(size_t)smem + L_ML + (lane & 7) * 264 + rm * 8, mlw = (unsigned)(size_t)smem + L_ML + wave * 264 + col * 8;
  unsigned oa = (unsigned)(size_t)smem + (lane & 16) * 512 + (r0 + rh) * 256 + 16 * ((lane & 15) ^ ((r0 + rh) & 15)) + (fin ? 8 * hh : 0), oa2 = oa + 65536;
  unsigned sa = (r0 + rh) * (MAXS * REC) + (fin ? WS_REC + lane * 16 : col * 16);
  unsigned so = WS_REC + (np - 1 - slot) * REC, so1 = so + 16 * MAXS * REC;
  unsigned outa = ((be * NH + head * 4 + (wave & 3)) * DH + 4 * col + 2 * hh) * 2;
  unsigned loca = (unsigned)(size_t)smem + L_TAB + (wave & 3) * 1024 + lane * 16;
  asm volatile("" : "+v"(tg0), "+v"(tg1), "+v"(th0), "+v"(th1), "+v"(mla), "+v"(mlw), "+v"(oa), "+v"(oa2), "+v"(sa), "+v"(outa), "+v"(loca));
  u32x4 srdW = {(unsigned)wsb, (unsigned)(wsb >> 32) & 0xffffu, WS_HEAD, 0x00020000u};
  asm volatile("" : "+s"(so), "+s"(so1), "+s"(ctag), "+s"(srdW));

  {
    if (tot & 1) WAITVM(4); else WAITVM(8);
    if (GPOS == 1) count(IC<0>{});
    bf16x8 kf[8];
    readk(kf);
    __builtin_amdgcn_sched_barrier(0);
    scores(kf);
    *(__attribute__((address_space(3))) f32x2*)mlw = f32x2{ms, swap32_sum(lsum)};
    auto keep = [&](int c) {
#pragma unroll
      for (int g = 0; g < 4; ++g)
        *(__attribute__((address_space(3))) f32x4*)(ka8[4 * (c & 1) + g] + (c >> 1) * 8192) = f32x4{acc[c][4 * g], acc[c][4 * g + 1], acc[c][4 * g + 2], acc[c][4 * g + 3]};
      __builtin_amdgcn_sched_barrier(0);
    };
    bf16x8 vf[4][2];
#pragma unroll
    for (int s = 0; s < 2; ++s) {
      if (s == 1 || (tot & 1)) WAITVM(0); else WAITVM(4);
      if (GPOS == 2 && s == 0) count(IC<0>{});
#pragma unroll
      for (int c = 0; c < 4; ++c) {
        const bf16x4 x = __builtin_amdgcn_ds_read_tr16_b64_v4bf16((lds_bf16x4_p)(va4[c] + s * 4096));
        const bf16x4 y = __builtin_amdgcn_ds_read_tr16_b64_v4bf16((lds_bf16x4_p)(va4[c] + s * 4096 + 2048));
        vf[c][s] = __builtin_shufflevector(x, y, 0, 1, 2, 3, 4, 5, 6, 7);
      }
      __builtin_amdgcn_sched_barrier(0);
#pragma unroll
      for (int c = 0; c < 4; ++c) {
        acc[c] = __builtin_amdgcn_mfma_f32_32x32x16_bf16(vf[c][s], pf[s], acc[c], 0, 0, 0);
        __builtin_amdgcn_sched_barrier(0);
        if (s == 1 && c == 1) keep(0);
      }
    }
    STAMP(4);
    keep(1); keep(2);
    keep(3);
  }
  if (fin && wave < 4) {
    count(IC<4>{});
    u32x32 B;
    asm volatile("" : "={v[192:223]}"(B));
    f32x4 G;
    unsigned laps = 1u << 18;
#define ST(k)
#define ISSUE(j) \
        "s_ff1_i32_b64 s59, %2\n\t" \
        "s_bitset0_b64 %2, s59\n\t" \
        "s_lshl_b32 s[64+" #j "], s59, 10\n\t" \
        "buffer_load_dwordx4 v[64+4*" #j ":64+4*" #j "+3], %6, %7, s[64+" #j "] offen sc1\n\t"
#define BULK(j) \
        "s_waitcnt vmcnt(31-" #j ")\n\t" \
        "v_xor_b32_dpp v232, v[64+4*" #j "+1], v[64+4*" #j "+3] quad_perm:[2,3,0,1] row_mask:0xf bank_mask:0xf\n\t" \
        "v_cmp_ne_u32 vcc, %9, v232\n\t" \
        "v_xor_b32_dpp v[224+" #j "/4], v[64+4*" #j "+1], v233 quad_perm:[0,1,2,3] row_mask:0xf bank_mask:(1<<(" #j "&3))\n\t" \
        "s_cmp_lg_u64 vcc, 0\n\t" \
        "s_addc_u32 s60, s60, s60\n\t"
#define STEP(k) \
        "s_waitcnt vmcnt(7)\n\t" \
        "s_bitcmp1_b64 s[60:61], s[48+" #k "]\n\t" \
        "s_cbranch_scc0 1f\n\t" \
        "v_xor_b32_dpp v232, v[192+4*" #k "+1], v[192+4*" #k "+3] quad_perm:[2,3,0,1] row_mask:0xf bank_mask:0xf\n\t" \
        "v_cmp_ne_u32 vcc, %9, v232\n\t" \
        "s_lshl_b32 s59, s[48+" #k "], 2\n\t" \
        "s_lshr_b32 s58, s[48+" #k "], 2\n\t" \
        "s_cbranch_vccnz 1f\n\t" \
        "s_set_gpr_idx_on s59, gpr_idx(DST)\n\t" \
        "s_and_b32 s59, s59, 12\n\t" \
        "v_mov_b32 v64, v[192+4*" #k "]\n\t" \
        "v_mov_b32 v66, v[192+4*" #k "+2]\n\t" \
        "s_set_gpr_idx_idx s58\n\t" \
        "s_lshl_b64 exec, s[62:63], s59\n\t" \
        "v_xor_b32 v224, v233, v[192+4*" #k "+1]\n\t" \
        "s_set_gpr_idx_off\n\t" \
        "s_mov_b64 exec, -1\n\t" \
        "s_bitset0_b32 s60, s[48+" #k "]\n\t" \
        "s_cmp_eq_u32 s60, 0\n\t" \
        "s_cbranch_scc1 7f\n\t" \
        "1:\n\t" \
        "s_and_b32 s56, s56, s60\n\t" \
        "s_cselect_b32 s56, s56, s60\n\t" \
        "s_ff1_i32_b32 s[48+" #k "], s56\n\t" \
        "s_mov_b32 m0, s[48+" #k "]\n\t" \
        "s_bitset0_b32 s56, s[48+" #k "]\n\t" \
        "s_movrels_b32 s59, s64\n\t" \
        "s_cmp_lg_u32 s56, 0\n\t" \
        "buffer_load_dwordx4 v[192+4*" #k ":192+4*" #k "+3], %6, %7, s59 offen sc1\n\t" \
        "s_cbranch_scc1 1f\n\t" \
        "s_sleep %13\n\t" \
        "1:\n\t"
#define WEIGH(g) \
        "v_sub_f32_dpp v[234+(" #g "&1)], v[224+" #g "], v233 quad_perm:[0,0,0,0] row_mask:0xf bank_mask:0xf\n\t" \
        "v_exp_f32 v[234+(" #g "&1)], v[234+(" #g "&1)]\n\t"
#define ADD1(g, i) \
        "v_fmac_f32_dpp v[238+2*(" #i "&1)], v[234+(" #g "&1)], v[64+16*" #g "+4*" #i "] row_newbcast:4*" #i " row_mask:0xf bank_mask:0xf\n\t" \
        "v_fmac_f32_dpp v[239+2*(" #i "&1)], v[234+(" #g "&1)], v[64+16*" #g "+4*" #i "+2] row_newbcast:4*" #i " row_mask:0xf bank_mask:0xf\n\t"
#define ADD(g) \
        "s_cmp_ge_u32 s57, 4*" #g "\n\t" \
        "v_fmac_f32 v242, v[234+(" #g "&1)], v[224+" #g "]\n\t" \
        ADD1(g, 3) ADD1(g, 2) ADD1(g, 1) ADD1(g, 0) \
        "s_cbranch_scc1 8f\n\t"
    asm volatile(
        "s_setprio " STR(CP) "\n\t"
        "s_getpc_b64 s[52:53]\n\t"
        "9:\n\t"
        "s_bcnt1_i32_b64 s57, %2\n\t"
        "s_sub_u32 s57, 32, s57\n\t"
        "s_mul_i32 s59, s57, (3f-2f)/32\n\t"
        "s_add_u32 s59, s59, 2f-9b\n\t"
        "s_add_u32 s54, s52, s59\n\t"
        "s_addc_u32 s55, s53, 0\n\t"
        "s_mul_i32 s59, s57, (5f-4f)/32\n\t"
        "s_add_u32 s59, s59, 4f-9b\n\t"
        "s_add_u32 s52, s52, s59\n\t"
        "s_addc_u32 s53, s53, 0\n\t"
        "s_and_b32 s59, s57, 28\n\t"
        "s_lshl_b32 s59, s59, 2\n\t"
        "s_set_gpr_idx_on s59, gpr_idx(DST)\n\t"
        "s_mov_b64 s[60:61], 0\n\t"
        "v_mov_b32 v64, 0\n\tv_mov_b32 v66, 0\n\tv_mov_b32 v68, 0\n\tv_mov_b32 v70, 0\n\tv_mov_b32 v72, 0\n\tv_mov_b32 v74, 0\n\t"
        "s_set_gpr_idx_off\n\t"
        "s_mov_b32 s62, 0x000f000f\n\t"
        "s_mov_b32 s63, s62\n\t"
        "v_mov_b32 v224, %10\n\tv_mov_b32 v225, %10\n\tv_mov_b32 v226, %10\n\tv_mov_b32 v227, %10\n\t"
        "v_mov_b32 v228, %10\n\tv_mov_b32 v229, %10\n\tv_mov_b32 v230, %10\n\tv_mov_b32 v231, %10\n\t"
        "v_mov_b32 v233, %8\n\tv_mov_b32 v240, 0\n\tv_mov_b32 v241, 0\n\tv_mov_b32 v242, 0\n\t"
        "1:\n\t"
        "ds_read_b32 v232, %12\n\t"
        "s_waitcnt lgkmcnt(0)\n\t"
        "v_cmp_gt_u32 vcc, %14, v232\n\t"
        "s_cbranch_vccz 0f\n\t"
        "s_sleep 1\n\t"
        "s_branch 1b\n\t"
        "0:\n\t"
        ST(4)
        "s_setpc_b64 s[54:55]\n\t"
        "2:\n\t"
        ISSUE(0) ISSUE(1) ISSUE(2) ISSUE(3) ISSUE(4) ISSUE(5) ISSUE(6) ISSUE(7)
        ISSUE(8) ISSUE(9) ISSUE(10) ISSUE(11) ISSUE(12) ISSUE(13) ISSUE(14) ISSUE(15)
        ISSUE(16) ISSUE(17) ISSUE(18) ISSUE(19) ISSUE(20) ISSUE(21) ISSUE(22) ISSUE(23)
        ISSUE(24) ISSUE(25) ISSUE(26) ISSUE(27) ISSUE(28) ISSUE(29) ISSUE(30) ISSUE(31)
        "3:\n\t"
        "s_setpc_b64 s[52:53]\n\t"
        "4:\n\t"
        BULK(0) BULK(1) BULK(2) BULK(3) BULK(4) BULK(5) BULK(6) BULK(7)
        BULK(8) BULK(9) BULK(10) BULK(11) BULK(12) BULK(13) BULK(14) BULK(15)
        BULK(16) BULK(17) BULK(18) BULK(19) BULK(20) BULK(21) BULK(22) BULK(23)
        BULK(24) BULK(25) BULK(26) BULK(27) BULK(28) BULK(29) BULK(30) BULK(31)
        "5:\n\t"
        "s_brev_b32 s60, s60\n\t"
        "s_cmp_eq_u32 s60, 0\n\t"
        "s_cbranch_scc1 7f\n\t"
        "s_mov_b32 s48, 63\n\ts_mov_b32 s49, 63\n\ts_mov_b64 s[50:51], s[48:49]\n\ts_mov_b64 s[52:53], s[48:49]\n\ts_mov_b64 s[54:55], s[48:49]\n\t"
        "s_mov_b32 s56, s60\n\t"
        "6:\n\t"
        STEP(0) STEP(1) STEP(2) STEP(3) STEP(4) STEP(5) STEP(6) STEP(7)
        "s_sub_u32 %3, %3, 1\n\t"
        "s_cbranch_scc0 6b\n\t"
        "7:\n\t"
        ST(5)
        "s_setprio " STR(CP2) "\n\t"
        "s_barrier\n\t"
        "ds_read_b128 v[236:239], %11\n\t"
        "v_max3_f32 v232, v224, v225, v226\n\t"
        "v_max3_f32 v233, v227, v228, v229\n\t"
        "v_max3_f32 v232, v232, v230, v231\n\t"
        "v_max_f32 v232, v232, v233\n\t"
        "s_nop 1\n\t"
        "v_max_f32_dpp v232, v232, v232 row_ror:4 row_mask:0xf bank_mask:0xf\n\t"
        "s_nop 1\n\t"
        "v_max_f32_dpp v232, v232, v232 row_ror:8 row_mask:0xf bank_mask:0xf\n\t"
        "s_waitcnt lgkmcnt(0)\n\t"
        "s_nop 0\n\t"
        "v_max_f32_dpp v233, v232, v236 quad_perm:[0,0,0,0] row_mask:0xf bank_mask:0xf\n\t"
        "v_sub_f32 v236, v236, v233\n\t"
        "v_exp_f32 v236, v236\n\t"
        WEIGH(7)
        "v_pk_mul_f32 v[238:239], v[238:239], v[236:237] op_sel_hi:[1,0]\n\t"
        "v_mul_f32 v237, v237, v236\n\t"
        WEIGH(6) ADD(7) WEIGH(5) ADD(6) WEIGH(4) ADD(5) WEIGH(3) ADD(4) WEIGH(2) ADD(3) WEIGH(1) ADD(2) WEIGH(0) ADD(1) ADD(0)
        "8:\n\t"
        "v_add_f32_dpp v242, v242, v242 row_ror:4 row_mask:0xf bank_mask:0xf\n\t"
        "v_pk_add_f32 v[238:239], v[238:239], v[240:241]\n\t"
        "s_nop 0\n\t"
        "v_add_f32_dpp v242, v242, v242 row_ror:8 row_mask:0xf bank_mask:0xf\n\t"
        "s_nop 1\n\t"
        "v_add_f32_dpp v237, v242, v237 quad_perm:[1,1,1,1] row_mask:0xf bank_mask:0xf\n\t"
        "v_rcp_f32 v236, v237\n\t"
        "s_nop 0"
        : "+{v[192:223]}"(B), "={v[236:239]}"(G), "+s"(pend), "+s"(laps),
          "+s"(so), "+s"(so1)
        : "v"(sa), "s"(srdW), "s"(ctag), "s"(ctag ^ (ctag << 1)), "v"((lane & 1) ? 0.f : LOWEST), "v"(loca), "v"((unsigned)(size_t)smem + L_GC), "n"(PACE), "n"(GATE)
        : "vcc", "scc", "memory", CLOBBER);
    STAMP(5);
    const f32x2 a = f32x2{G[2], G[3]} * G[0];
    const bf16x2 ov = __builtin_convertvector(a, bf16x2);
    asm volatile("s_nop 1\n\tglobal_store_dword %0, %1, %2 sc1" :: "v"(outa), "v"(ov), "s"(ob) : "memory");
    asm volatile("" :: "{v[192:223]}"(B) : "memory");
  } else if (fin) {
    count(IC<4>{});
    await(4, NW);
    STAMP(1);
    const f32x2 ml = *(const __attribute__((address_space(3))) f32x2*)mla;
    __builtin_amdgcn_sched_barrier(0);
    f32x2 o[NW];
#pragma unroll
    for (int w = 0; w < NW; ++w) {
      o[w] = *(const __attribute__((address_space(3))) f32x2*)((w < 4 ? oa : oa2) + (w & 3) * 16384);
      __builtin_amdgcn_sched_barrier(0);
    }
    float M, f;
    asm volatile(
        "v_max_f32_dpp %0, %2, %2 quad_perm:[1,0,3,2] row_mask:0xf bank_mask:0xf\n\t"
        "s_nop 1\n\t"
        "v_max_f32_dpp %0, %0, %0 quad_perm:[2,3,0,1] row_mask:0xf bank_mask:0xf\n\t"
        "s_nop 1\n\t"
        "v_max_f32_dpp %0, %0, %0 row_half_mirror row_mask:0xf bank_mask:0xf\n\t"
        "v_sub_f32 %1, %2, %0\n\t"
        "v_exp_f32 %1, %1\n\t"
        "s_nop 1"
        : "=&v"(M), "=&v"(f) : "v"(ml[0]));
    float L = ml[1] * f;
    L += dppf<0xb1>(L); L += dppf<0x4e>(L); L += dppf<0x141>(L);
    f32x2 a = o[0] * dppf<0x150>(f);
    a += o[1] * dppf<0x151>(f); a += o[2] * dppf<0x152>(f); a += o[3] * dppf<0x153>(f);
    a += o[4] * dppf<0x154>(f); a += o[5] * dppf<0x155>(f); a += o[6] * dppf<0x156>(f); a += o[7] * dppf<0x157>(f);
    *(__attribute__((address_space(3))) f32x4*)loca = f32x4{M, L, a[0], a[1]};
    STAMP(3);
    asm volatile("s_waitcnt lgkmcnt(0)\n\ts_barrier" ::: "memory");
    if (lane == 0) __builtin_amdgcn_raw_buffer_store_b32(ctag, __builtin_amdgcn_make_buffer_rsrc((void*)wsb, 0, WS_HEAD, 0x00020000), crow * 4, 0, 16);
  } else {
  asm volatile("s_waitcnt lgkmcnt(0)\n\ts_barrier" ::: "memory");
  auto merge = [&](auto NP) {
    constexpr int np = decltype(NP)::value;
    const f32x2 ml = *(const __attribute__((address_space(3))) f32x2*)mla;
    __builtin_amdgcn_sched_barrier(0);
    f32x4 o[np][NW];
#pragma unroll
    for (int p = 0; p < np; ++p)
#pragma unroll
      for (int w = 0; w < NW; ++w) {
        o[p][w] = *(const __attribute__((address_space(3))) f32x4*)((w < 4 ? oa : oa2) + p * 4096 + (w & 3) * 16384);
        __builtin_amdgcn_sched_barrier(0);
      }
    float M, f;
    asm volatile(
        "v_max_f32_dpp %0, %2, %2 quad_perm:[1,0,3,2] row_mask:0xf bank_mask:0xf\n\t"
        "s_nop 1\n\t"
        "v_max_f32_dpp %0, %0, %0 quad_perm:[2,3,0,1] row_mask:0xf bank_mask:0xf\n\t"
        "s_nop 1\n\t"
        "v_max_f32_dpp %0, %0, %0 row_half_mirror row_mask:0xf bank_mask:0xf\n\t"
        "v_sub_f32 %1, %2, %0\n\t"
        "v_exp_f32 %1, %1\n\t"
        "s_nop 1"
        : "=&v"(M), "=&v"(f) : "v"(ml[0]));
    float L = ml[1] * f;
    L += dppf<0xb1>(L); L += dppf<0x4e>(L); L += dppf<0x141>(L);
    const int st = __float_as_int(__builtin_amdgcn_inverse_ballot_w64(0xaaaaaaaaaaaaaaaaull) ? L : M);
    {
      f32x4 a = o[0][0] * dppf<0x150>(f);
      a += o[0][1] * dppf<0x151>(f); a += o[0][2] * dppf<0x152>(f); a += o[0][3] * dppf<0x153>(f);
      a += o[0][4] * dppf<0x154>(f); a += o[0][5] * dppf<0x155>(f); a += o[0][6] * dppf<0x156>(f); a += o[0][7] * dppf<0x157>(f);
      const unsigned s0 = __builtin_amdgcn_update_dpp(st, st, 0x118, 0xf, 0xf, false), w0 = s0 ^ tg0, w1 = s0 ^ th0;
      put2(u32x4{__float_as_uint(a[0]), w0, __float_as_uint(a[1]), w1}, u32x4{__float_as_uint(a[2]), w0, __float_as_uint(a[3]), w1}, sa, srdW, so);
    }
    if constexpr (np == 2) {
      f32x4 a = o[1][0] * dppf<0x158>(f);
      a += o[1][1] * dppf<0x159>(f); a += o[1][2] * dppf<0x15a>(f); a += o[1][3] * dppf<0x15b>(f);
      a += o[1][4] * dppf<0x15c>(f); a += o[1][5] * dppf<0x15d>(f); a += o[1][6] * dppf<0x15e>(f); a += o[1][7] * dppf<0x15f>(f);
      const unsigned s1 = __builtin_amdgcn_update_dpp(st, st, 0x108, 0xf, 0xf, false), w0 = s1 ^ tg1, w1 = s1 ^ th1;
      put2(u32x4{__float_as_uint(a[0]), w0, __float_as_uint(a[1]), w1}, u32x4{__float_as_uint(a[2]), w0, __float_as_uint(a[3]), w1}, sa, srdW, so1);
    }
  };
  {
    if (actm == 3) merge(IC<2>{}); else if (actm != 0) merge(IC<1>{});
    STAMP(5);
  }
  }
}

extern "C" void kernel_launch(void* const* d_in, const int* in_sizes, int n_in, void* d_out, int out_size, void* d_ws, size_t ws_size,
                              hipStream_t stream) {
  const unsigned kv_bytes = (unsigned)in_sizes[1] * 2u;
  attn_main<<<256, NTHR, 0, stream>>>(d_in[0], d_in[1], d_in[2], d_in[3], d_in[4], d_ws, d_out, kv_bytes, 0u);
}
```
